```python
import torch

def _softmax(x, axis=-1):
    return torch.softmax(x, dim=axis)

def _gelu(x):
    return torch.nn.functional.gelu(x, approximate='tanh')

def _silu(x):
    return torch.nn.functional.silu(x)

def _sigmoid(x):
    return torch.sigmoid(x)

def _layernorm(x, axis=-1, eps=1e-05):
    m = x.mean(dim=axis, keepdim=True)
    v = x.var(dim=axis, keepdim=True, unbiased=False)
    return (x - m) / torch.sqrt(v + eps)

def _scatter_add(tgt, idx, src, axis=0):
    return tgt.clone().index_add_(axis, idx, src)

def _segment_sum(src, seg, n):
    out = torch.zeros((n,) + src.shape[1:], dtype=src.dtype)
    return out.index_add_(0, seg, src)

def _pad(x, pw):
    flat = tuple((v for pair in reversed(pw) for v in pair))
    return torch.nn.functional.pad(x, flat)
kernel_name = 'gated_mix__memory_aug_recurrent_mem_tr__spectral_gfnet__prompt'

def setup_inputs():

    _w = lambda a: (lambda t: t.to(torch.bfloat16) if t.is_floating_point() else t)(torch.as_tensor(a))

    def _A_setup():
        B, S, Dm = (1, 2048, 448)
        H, Dh = (16, 256)
        M, seg = (8, 32)
        seg = min(seg, S)
        S = S // seg * seg
        return {'x': _w((lambda *s: torch.randn(*s, dtype=torch.bfloat16) * 0.05)(B, S, Dm)), 'mem0': _w((lambda *s: torch.randn(*s, dtype=torch.bfloat16) * 0.4)(B, M, Dm)), 'Wq': _w((lambda *s: torch.randn(*s, dtype=torch.bfloat16) * 6.4)(Dm, H, Dh) * Dm ** (-0.5)), 'Wk': _w((lambda *s: torch.randn(*s, dtype=torch.bfloat16) * 6.4)(Dm, H, Dh) * Dm ** (-0.5)), 'Wv': _w((lambda *s: torch.randn(*s, dtype=torch.bfloat16) * 0.4)(Dm, H, Dh) * Dm ** (-0.5)), 'Wo': _w((lambda *s: torch.randn(*s, dtype=torch.bfloat16) * 0.4)(H, Dh, Dm) * (H * Dh) ** (-0.5)), 'seg_len': seg}
    _A_in = _A_setup()

    def _B_setup():
        B, S, D = (1, 2048, 448)
        Df = D // 2 + 1
        return {'x': _w((lambda *s: torch.randn(*s, dtype=torch.bfloat16) * 0.05)(B, S, D)), 'w_r': _w((lambda *s: torch.randn(*s, dtype=torch.bfloat16) * 3.2)(S, Df) * 0.02), 'w_i': _w((lambda *s: torch.randn(*s, dtype=torch.bfloat16) * 3.2)(S, Df) * 0.02)}
    _B_in = _B_setup()
    return {'A_x': _A_in['x'], 'A_mem0': _A_in['mem0'], 'A_Wq': _A_in['Wq'], 'A_Wk': _A_in['Wk'], 'A_Wv': _A_in['Wv'], 'A_Wo': _A_in['Wo'], 'A_seg_len': _A_in['seg_len'], 'B_x': _B_in['x'], 'B_w_r': _B_in['w_r'], 'B_w_i': _B_in['w_i'], 'gate_w': _w((lambda *s: torch.randn(*s, dtype=torch.bfloat16) * 0.05)(448) * 16.0)}

def reference(A_x, A_mem0, A_Wq, A_Wk, A_Wv, A_Wo, A_seg_len, B_x, B_w_r, B_w_i, gate_w):

    def _A_ref(x, mem0, Wq, Wk, Wv, Wo, seg_len):
        B, S, Dm = x.shape
        H, Dh = (Wq.shape[1], Wq.shape[2])
        M = mem0.shape[1]
        scale = Dh ** (-0.5)
        n_seg = S // seg_len
        L = M + seg_len
        idx = torch.arange(L)
        mask = (idx[:, None] < M) | (idx[:, None] >= idx[None, :])
        mem = mem0
        outs = []
        for s in range(n_seg):
            xs = x[:, s * seg_len:(s + 1) * seg_len, :].clone()
            h = torch.concatenate([mem, xs], axis=1)
            q = torch.einsum('btd,dhe->bhte', h, Wq)
            k = torch.einsum('btd,dhe->bhte', h, Wk)
            v = torch.einsum('btd,dhe->bhte', h, Wv)
            scores = torch.einsum('bhqe,bhke->bhqk', q, k) * scale
            scores = torch.where(mask[None, None], scores, -1000000000.0)
            p = _softmax(scores, axis=-1)
            o = torch.einsum('bhqk,bhke->bhqe', p, v)
            o = torch.einsum('bhte,hed->btd', o, Wo)
            mem = o[:, :M, :].clone()
            outs.append(o[:, M:, :])
        return torch.concatenate(outs, axis=1)

    def _B_ref(x, w_r, w_i):
        B, S, D = x.shape
        X = (lambda x, axes=(-2, -1): torch.fft.rfft2(x.float(), dim=axes))(x, axes=(-2, -1))
        W = w_r + 1j * w_i
        X = X * W
        y = (lambda x, s, axes=(-2, -1): torch.fft.irfft2(x, s=s, dim=axes).to(torch.bfloat16))(X, (S, D), axes=(-2, -1))
        return y
    x = A_x
    g = _sigmoid((x * gate_w).sum(-1, keepdims=True))
    a = _A_ref(x, A_mem0, A_Wq, A_Wk, A_Wv, A_Wo, A_seg_len)
    b = _B_ref(x, B_w_r, B_w_i)
    return g * a + (1 - g) * b
```

```cpp
#include <hip/hip_runtime.h>
#include <hip/hip_bf16.h>
#include <stdint.h>

typedef __attribute__((ext_vector_type(8))) __bf16 bf8;
typedef __attribute__((ext_vector_type(2))) __bf16 bf2;
typedef __attribute__((ext_vector_type(4))) float f4;
typedef __attribute__((ext_vector_type(2))) float f2;
typedef __attribute__((ext_vector_type(4))) unsigned u4;
typedef __attribute__((ext_vector_type(2))) unsigned u2;
typedef __attribute__((ext_vector_type(4))) short s4;
typedef unsigned short us;
typedef __amdgpu_buffer_rsrc_t rsrc_t;
#define DEV __device__ __forceinline__

constexpr int SLEN = 2048, DM = 448, NH = 16, NSEG = 64;
constexpr int NA = 80, NX = 8, NB = 28, NGV = 56, NGW = 84;
constexpr unsigned KB = 1024;
constexpr unsigned O_AF = 0;
constexpr unsigned O_BF = O_AF + 16 * 14 * 2 * 14 * KB;
constexpr unsigned O_GF = O_BF + 16 * 28 * 14 * KB;
constexpr unsigned O_VF = O_GF + 64 * 16 * 28 * KB;
constexpr unsigned O_SXX = O_VF + 64 * 16 * 28 * KB;
constexpr unsigned O_SXM = O_SXX + 64 * 16 * 4 * KB;
constexpr unsigned O_T2F = O_SXM + 64 * 80 * KB;
constexpr unsigned O_FW = O_T2F + 64 * 28 * 16 * 256;
constexpr unsigned O_FI = O_FW + 15 * 4 * 14 * KB;
constexpr unsigned O_WT = O_FI + 28 * 2 * 15 * KB;
constexpr unsigned O_Y1 = O_WT + 57 * 2048 * 16;
constexpr unsigned O_Y2 = O_Y1 + 2 * 240 * 2048 * 4;
constexpr unsigned O_GATE = O_Y2 + 128 * 15 * 2 * KB;
constexpr unsigned O_FL = O_GATE + 2048 * 4;
constexpr unsigned FL_G = O_FL;
constexpr unsigned FL_V = FL_G + 64 * 128;
constexpr unsigned FL_A = FL_V + 64 * 128;
constexpr unsigned FL_B = FL_A + 64 * 512;
constexpr unsigned FL_X1 = FL_B + 64 * 128;
constexpr unsigned FL_1 = FL_X1 + 64 * 128;
constexpr unsigned FL_2 = FL_1 + 256;
constexpr unsigned FL_END = FL_2 + 256;
constexpr unsigned O_PAR = FL_END;
constexpr unsigned O_DBG = O_PAR + 256;
constexpr unsigned O_EX = ((O_DBG + 65536 + (2u << 20) - 1) >> 21) << 21;
constexpr unsigned WS_NEED = O_EX + (4u << 20);
constexpr unsigned SENT = 0xFFFFFFFFu;
constexpr unsigned TAG = 1u;
constexpr unsigned SPIN_CAP = 1u << 17;
constexpr int LDS_BYTES = 128 * 1024;

struct Params {
    const us* x; const us* mem0; const us* wq; const us* wk; const us* wv; const us* wo;
    const us* wr; const us* wi; const us* gw; us* out; unsigned char* ws;
};

#define TS(slot, idx) do { } while (0)
DEV unsigned pk2(float lo, float hi) { f2 v = {lo, hi}; bf2 b = __builtin_convertvector(v, bf2); return __builtin_bit_cast(unsigned, b); }
DEV float bflo(unsigned v) { return __builtin_bit_cast(float, v << 16); }
DEV float bfhi(unsigned v) { return __builtin_bit_cast(float, v & 0xFFFF0000u); }
DEV float bf2f(us v) { return __builtin_bit_cast(float, (unsigned)v << 16); }
DEV f4 mfma32(u4 a, u4 b, f4 c) { return __builtin_amdgcn_mfma_f32_16x16x32_bf16(__builtin_bit_cast(bf8, a), __builtin_bit_cast(bf8, b), c, 0, 0, 0); }
DEV f4 mfma16(u2 a, u2 b, f4 c) { return __builtin_amdgcn_mfma_f32_16x16x16bf16_1k(__builtin_bit_cast(s4, a), __builtin_bit_cast(s4, b), c, 0, 0, 0); }
DEV u4 ld16c(rsrc_t r, unsigned off) { return __builtin_amdgcn_raw_buffer_load_b128(r, off, 0, 16); }
DEV u2 ld8c(rsrc_t r, unsigned off) { return __builtin_amdgcn_raw_buffer_load_b64(r, off, 0, 16); }
DEV unsigned ld4c(rsrc_t r, unsigned off) { return __builtin_amdgcn_raw_buffer_load_b32(r, off, 0, 16); }
DEV u4 ld16p(rsrc_t r, unsigned off) { return __builtin_amdgcn_raw_buffer_load_b128(r, off, 0, 0); }
DEV void st16c(rsrc_t r, unsigned off, u4 v) { __builtin_amdgcn_raw_buffer_store_b128(v, r, off, 0, 16); }
DEV void st8c(rsrc_t r, unsigned off, u2 v) { __builtin_amdgcn_raw_buffer_store_b64(v, r, off, 0, 16); }
DEV void st4c(rsrc_t r, unsigned off, unsigned v) { __builtin_amdgcn_raw_buffer_store_b32(v, r, off, 0, 16); }
DEV void vm0() { asm volatile("s_waitcnt vmcnt(0)" ::: "memory"); }
DEV void bar() { __syncthreads(); }
DEV float fu(unsigned v) { return __builtin_bit_cast(float, v); }
DEV unsigned uf(float v) { return __builtin_bit_cast(unsigned, v); }
DEV float ex2(float v) { return __builtin_amdgcn_exp2f(v); }
DEV float x16max(float v) { auto r = __builtin_amdgcn_permlane16_swap(uf(v), uf(v), false, false); return fmaxf(fu(r[0]), fu(r[1])); }
DEV float x32max(float v) { auto r = __builtin_amdgcn_permlane32_swap(uf(v), uf(v), false, false); return fmaxf(fu(r[0]), fu(r[1])); }
DEV float x16add(float v) { auto r = __builtin_amdgcn_permlane16_swap(uf(v), uf(v), false, false); return fu(r[0]) + fu(r[1]); }
DEV float x32add(float v) { auto r = __builtin_amdgcn_permlane32_swap(uf(v), uf(v), false, false); return fu(r[0]) + fu(r[1]); }
DEV unsigned ror8(unsigned v) { return (unsigned)__builtin_amdgcn_update_dpp(0, (int)v, 0x128, 0xf, 0xf, false); }
DEV u4 ror8(u4 v) { u4 r = {ror8(v.x), ror8(v.y), ror8(v.z), ror8(v.w)}; return r; }
DEV unsigned nosent(unsigned v) { return v < 0xFFFFFFFEu ? v : 0xFFFFFFFEu; }
DEV bool ok4(u4 v) { return v.x != SENT && v.y != SENT && v.z != SENT && v.w != SENT; }
DEV bool ok2(u2 v) { return v.x != SENT && v.y != SENT; }
DEV unsigned exoff(int s, int b, unsigned par2) { return O_EX + (unsigned)(s * 4 + b) * 16384u + ((par2 >> (s >> 5)) & 1u) * 8192u; }
DEV u4 ldx16(const us* p) { return *(const u4*)p; }

DEV bool wait_flags(rsrc_t r, unsigned off, int n, int lane) {
    const unsigned o0 = off + 4u * (lane < n ? lane : 0), o1 = off + 4u * (lane + 64 < n ? lane + 64 : 0);
    for (unsigned it = 0; it < SPIN_CAP; ++it) {
        asm volatile("" ::: "memory");
        unsigned a = ld4c(r, o0), b = TAG;
        if (n > 64) b = ld4c(r, o1);
        if (__all(a == TAG && b == TAG)) return true;
        __builtin_amdgcn_s_sleep(1);
    }
    return false;
}

DEV int bitrev11(int p) { return (int)(__builtin_bitreverse32((unsigned)p) >> 21); }

__global__ void __launch_bounds__(256, 1) prep_kernel(Params P) {
    extern __shared__ __attribute__((aligned(16))) unsigned char lds[];
    const int b = blockIdx.x, tid = threadIdx.x, lane = tid & 63, w = __builtin_amdgcn_readfirstlane(tid >> 6);
    const int n = lane & 15, g = lane >> 4;
    rsrc_t rs = __builtin_amdgcn_make_buffer_rsrc(P.ws, 0, 0x10000000, 0x00020000);
    {
        const u4 sv = {SENT, SENT, SENT, SENT};
        const int s = b >> 2, blk = b & 3;
        const unsigned len = blk == 0 ? 7168u : (blk == 1 ? 8192u : 5120u);
        for (int par = 0; par < 2; ++par) {
            const unsigned base = O_EX + (unsigned)(s * 4 + blk) * 16384u + par * 8192u;
            if (s == 0 && blk == 0) {
                for (unsigned o = tid * 16u; o < len; o += 4096u) {
                    const int ch = o >> 4, kg = ch >> 3, row = ch & 7;
                    u4 v = ldx16(P.mem0 + row * DM + kg * 8);
                    v.x = nosent(v.x); v.y = nosent(v.y); v.z = nosent(v.z); v.w = nosent(v.w);
                    *(u4*)(P.ws + base + o) = v;
                }
            } else {
                for (unsigned o = tid * 16u; o < len; o += 4096u) *(u4*)(P.ws + base + o) = sv;
            }
        }
        const unsigned nfl = (FL_END - O_FL) / 16;
        const u4 z = {0, 0, 0, 0};
        for (unsigned i = b * 256 + tid; i < nfl; i += 65536) *(u4*)(P.ws + O_FL + i * 16) = z;
    }
    if (b < 224) {
        const bool isA = b < 112;
        const int bb = isA ? b : b - 112;
        const int h = bb / 7, up = bb % 7;
        u4 Bop[4][8];
        if (isA) {
#pragma unroll
            for (int ct = 0; ct < 4; ++ct) {
                const int u = 2 * up + (ct >> 1), tt = ct & 1;
                const int c = 32 * u + 8 * (n >> 2) + 4 * tt + (n & 3);
#pragma unroll
                for (int q = 0; q < 8; ++q) Bop[ct][q] = ldx16(P.wq + ((size_t)c * NH + h) * 256 + 32 * q + 8 * g);
            }
        } else {
            const int f = 16 * (4 * up + w) + n;
            u4* L = (u4*)lds;
#pragma unroll
            for (int q = 0; q < 8; ++q) {
                unsigned t[8];
#pragma unroll
                for (int j = 0; j < 8; ++j) t[j] = P.wo[((size_t)h * 256 + 32 * q + 8 * g + j) * DM + f];
                u4 v = {t[0] | (t[1] << 16), t[2] | (t[3] << 16), t[4] | (t[5] << 16), t[6] | (t[7] << 16)};
                L[(w * 8 + q) * 64 + lane] = v;
            }
            bar();
#pragma unroll
            for (int ct = 0; ct < 4; ++ct)
#pragma unroll
                for (int q = 0; q < 8; ++q) Bop[ct][q] = L[(ct * 8 + q) * 64 + lane];
        }
        const us* W1 = isA ? P.wk : P.wv;
        const float sc = isA ? 0.0625f * 1.4426950408889634f : 1.0f;
        for (int k = w; k < 14; k += 4) {
            f4 acc[4][2];
#pragma unroll
            for (int ct = 0; ct < 4; ++ct) { acc[ct][0] = f4{0, 0, 0, 0}; acc[ct][1] = f4{0, 0, 0, 0}; }
            u4 Aop[2][8];
#pragma unroll
            for (int e = 0; e < 2; ++e) {
                const int d = 32 * k + 8 * (n >> 2) + 4 * e + (n & 3);
#pragma unroll
                for (int q = 0; q < 8; ++q) Aop[e][q] = ldx16(W1 + ((size_t)d * NH + h) * 256 + 32 * q + 8 * g);
            }
#pragma unroll
            for (int q = 0; q < 8; ++q)
#pragma unroll
                for (int ct = 0; ct < 4; ++ct)
#pragma unroll
                    for (int e = 0; e < 2; ++e) acc[ct][e] = mfma32(Aop[e][q], Bop[ct][q], acc[ct][e]);
#pragma unroll
            for (int ct = 0; ct < 4; ++ct) {
                u4 v = {pk2(acc[ct][0][0] * sc, acc[ct][0][1] * sc), pk2(acc[ct][0][2] * sc, acc[ct][0][3] * sc),
                        pk2(acc[ct][1][0] * sc, acc[ct][1][1] * sc), pk2(acc[ct][1][2] * sc, acc[ct][1][3] * sc)};
                unsigned off;
                if (isA) { const int u = 2 * up + (ct >> 1), tt = ct & 1; off = O_AF + (unsigned)(((h * 14 + u) * 2 + tt) * 14 + k) * KB; }
                else { const int ft = 4 * up + ct; off = O_BF + (unsigned)((h * 28 + ft) * 14 + k) * KB; }
                *(u4*)(P.ws + off + lane * 16) = v;
            }
        }
    } else {
        const int mb = b - 224;
        float* ct = (float*)lds;
        float* st = ct + 448;
        for (int i = tid; i < 448; i += 256) { float sv, cv; sincospif((float)i * (2.0f / 448.0f), &sv, &cv); ct[i] = cv; st[i] = sv; }
        bar();
        for (int id = mb * 4 + w; id < 840; id += 128) {
            float v[8];
            unsigned oh, ol;
            if (id < 420) {
                const int kt = id / 28, ri = (id / 14) & 1, ks = id % 14;
                const int k = 16 * kt + n;
#pragma unroll
                for (int j = 0; j < 8; ++j) {
                    const int d = 32 * ks + 8 * g + j;
                    const int m = (d * k) % 448;
                    v[j] = k <= 224 ? (ri == 0 ? ct[m] : -st[m]) : 0.0f;
                }
                oh = O_FW + (unsigned)(((kt * 2 + ri) * 2 + 0) * 14 + ks) * KB;
                ol = oh + 14 * KB;
            } else {
                const int id2 = id - 420, ft = id2 / 15, u = id2 % 15;
                const int d = 16 * ft + n;
#pragma unroll
                for (int j = 0; j < 8; ++j) {
                    const int k = 4 * (4 * u + g) + (j >> 1);
                    const int m = (d * k) % 448;
                    const float ck = (k == 0 || k == 224) ? 1.0f : 2.0f;
                    v[j] = k <= 224 ? ck * ((j & 1) == 0 ? ct[m] : -st[m]) : 0.0f;
                }
                oh = O_FI + (unsigned)((ft * 2 + 0) * 15 + u) * KB;
                ol = oh + 15 * KB;
            }
            u4 hi, lo;
            unsigned hh[4], ll[4];
#pragma unroll
            for (int j = 0; j < 4; ++j) {
                hh[j] = pk2(v[2 * j], v[2 * j + 1]);
                ll[j] = pk2(v[2 * j] - bflo(hh[j]), v[2 * j + 1] - bfhi(hh[j]));
            }
            hi = u4{hh[0], hh[1], hh[2], hh[3]}; lo = u4{ll[0], ll[1], ll[2], ll[3]};
            *(u4*)(P.ws + oh + lane * 16) = hi;
            *(u4*)(P.ws + ol + lane * 16) = lo;
        }
        bar();
        {
            unsigned* T = (unsigned*)lds;
            for (int r = w; r < 64; r += 4) {
                const int f = bitrev11(mb * 64 + r);
                for (int k = lane; k < 228; k += 64) {
                    unsigned v = 0;
                    if (k < 225) v = (unsigned)P.wr[f * 225 + k] | ((unsigned)P.wi[f * 225 + k] << 16);
                    T[r * 232 + k] = v;
                }
            }
            bar();
            for (int c = w; c < 57; c += 4) {
                u4 v = *(const u4*)(T + lane * 232 + 4 * c);
                *(u4*)(P.ws + O_WT + ((size_t)c * 2048 + mb * 64 + lane) * 16) = v;
            }
        }
        for (int r = 0; r < 16; ++r) {
            const int t = mb * 64 + w * 16 + r;
            float s = 0.f;
            if (lane < 56) {
                u4 xv = ldx16(P.x + t * DM + lane * 8), gv = ldx16(P.gw + lane * 8);
                s = bflo(xv.x) * bflo(gv.x) + bfhi(xv.x) * bfhi(gv.x) + bflo(xv.y) * bflo(gv.y) + bfhi(xv.y) * bfhi(gv.y)
                  + bflo(xv.z) * bflo(gv.z) + bfhi(xv.z) * bfhi(gv.z) + bflo(xv.w) * bflo(gv.w) + bfhi(xv.w) * bfhi(gv.w);
            }
            for (int o = 32; o > 0; o >>= 1) s += __shfl_xor(s, o);
            if (lane == 0) *(float*)(P.ws + O_GATE + t * 4) = 1.0f / (1.0f + __expf(-s));
        }
        for (int i = mb * 256 + tid; i < 128 * 2 * 48; i += 8192) {
            const int tb = i / 96, hl = (i / 48) & 1, e = i % 48;
            const u4 z = {0, 0, 0, 0};
            *(u4*)(P.ws + O_Y2 + (unsigned)((tb * 15 + 14) * 2 + hl) * KB + 256 + e * 16) = z;
        }
        if (b == 248 && w == 0) {
            for (int pg = 0; pg < 2; ++pg) {
                unsigned best[2] = {~0u, ~0u};
                for (int rep = 0; rep < 6; ++rep)
                    for (int par = 0; par < 2; ++par) {
                        const unsigned off = O_EX + (unsigned)(pg * 32 * 4) * 16384u + par * 8192u + 7168u + rep * 128u;
                        st4c(rs, off, 0u); vm0();
                        unsigned long long t0, t1; unsigned v;
                        const unsigned* gp = (const unsigned*)(P.ws + off);
                        asm volatile("s_memtime %0\n\ts_waitcnt lgkmcnt(0)\n\tglobal_load_dword %2, %3, off sc1\n\ts_waitcnt vmcnt(0)\n\ts_memtime %1\n\ts_waitcnt lgkmcnt(0)"
                                     : "=&s"(t0), "=&s"(t1), "=&v"(v) : "v"(gp) : "memory");
                        unsigned dt = (unsigned)(t1 - t0);
                        best[par] = dt < best[par] ? dt : best[par];
                    }
                if (lane == 0) { *(unsigned*)(P.ws + O_PAR + pg * 4) = best[1] < best[0] ? 1u : 0u;
                                 *(unsigned*)(P.ws + O_PAR + 16 + pg * 8) = best[0]; *(unsigned*)(P.ws + O_PAR + 20 + pg * 8) = best[1]; }
            }
        }
    }
}

DEV bool poll_mem(rsrc_t rs, unsigned base, int k0, int nk, int n, int g, u4& va, u4& vb) {
    const int ka = k0 + (n >> 3), kb = (k0 + 2 + (n >> 3) < k0 + nk) ? k0 + 2 + (n >> 3) : k0 + 2;
    const unsigned oa = base + (unsigned)((4 * ka + g) * 8 + (n & 7)) * 16u, ob = base + (unsigned)((4 * kb + g) * 8 + (n & 7)) * 16u;
    for (unsigned it = 0; it < SPIN_CAP; ++it) {
        asm volatile("" ::: "memory");
        va = ld16c(rs, oa); vb = ld16c(rs, ob);
        if (__all(ok4(va) && ok4(vb))) return true;
    }
    return false;
}
DEV void mem_to_lds(u4* memL, int k0, int nk, int n, int g, u4 va, u4 vb) {
    const int ka = k0 + (n >> 3), kb = k0 + 2 + (n >> 3);
    memL[(4 * ka + g) * 8 + (n & 7)] = va;
    if (kb < k0 + nk) memL[(4 * kb + g) * 8 + (n & 7)] = vb;
}

DEV void role_A(const Params& P, rsrc_t rs, unsigned char* lds, int ci, unsigned par2, int w, int lane) {
    const int n = lane & 15, g = lane >> 4;
    const int h = ci / 5, cj = ci % 5, u0 = 3 * cj, nu = cj < 4 ? 3 : 2;
    const int k0 = w < 2 ? 4 * w : 8 + 3 * (w - 2), nk = w < 2 ? 4 : 3;
    u4* memL = (u4*)lds;
    f4* red = (f4*)(lds + 16384);
    f4* red2 = (f4*)(lds + 16384 + 1024);
    u4 W[3][2][4];
#pragma unroll
    for (int uu = 0; uu < 3; ++uu)
#pragma unroll
        for (int tt = 0; tt < 2; ++tt)
#pragma unroll
            for (int kk = 0; kk < 4; ++kk) {
                u4 z = {0, 0, 0, 0};
                if (uu < nu && kk < nk) z = ld16p(rs, O_AF + (unsigned)(((h * 14 + u0 + uu) * 2 + tt) * 14 + k0 + kk) * KB + lane * 16);
                W[uu][tt][kk] = z;
            }
    bool alive = true;
    for (int s = 0; s < NSEG; ++s) {
        u4 XF[3][2];
#pragma unroll
        for (int uu = 0; uu < 3; ++uu)
#pragma unroll
            for (int tt = 0; tt < 2; ++tt) {
                const int u = uu < nu ? u0 + uu : u0;
                XF[uu][tt] = ldx16(P.x + (size_t)(32 * s + 16 * tt + n) * DM + 32 * u + 8 * g);
            }
        u4 va, vb;
        if (alive) alive = poll_mem(rs, exoff(s, 0, par2), k0, nk, n, g, va, vb);
        if (w == 0) TS(blockIdx.x, s * 8 + 0);
        u4* mL = memL + (s & 1) * 448;
        mem_to_lds(mL, k0, nk, n, g, va, vb);
        u4 F[4] = {va, ror8(va), vb, ror8(vb)};
        bar();
        u4 CF[3];
#pragma unroll
        for (int uu = 0; uu < 3; ++uu) { const int u = uu < nu ? u0 + uu : u0; CF[uu] = mL[(4 * u + g) * 8 + (n & 7)]; }
        u4 T[3];
        f4 sm = {0, 0, 0, 0};
#pragma unroll
        for (int uu = 0; uu < 3; ++uu) {
            f4 a0 = {0, 0, 0, 0}, a1 = {0, 0, 0, 0};
#pragma unroll
            for (int kk = 0; kk < 4; ++kk) { a0 = mfma32(W[uu][0][kk], F[kk], a0); a1 = mfma32(W[uu][1][kk], F[kk], a1); }
            T[uu] = u4{pk2(a0[0], a0[1]), pk2(a0[2], a0[3]), pk2(a1[0], a1[1]), pk2(a1[2], a1[3])};
            sm = mfma32(T[uu], CF[uu], sm);
        }
        if (s < NSEG - 1) {
            if (n < 8 && g < 2) red[w * 16 + g * 8 + n] = sm;
            bar();
            if (w == 0 && lane < 16) {
                f4 v = (red[lane] + red[16 + lane]) + (red[32 + lane] + red[48 + lane]);
                u2 o = {nosent(pk2(v[0], v[1])), nosent(pk2(v[2], v[3]))};
                const int e = h * 5 + cj;
                st8c(rs, exoff(s, 2 + (e >= 40), par2) + (unsigned)(e >= 40 ? e - 40 : e) * 128u + (lane & 7) * 16u + (lane >> 3) * 8u, o);
                TS(blockIdx.x, s * 8 + 1);
            }
        }
        f4 sx[2] = {f4{0, 0, 0, 0}, f4{0, 0, 0, 0}};
#pragma unroll
        for (int uu = 0; uu < 3; ++uu)
#pragma unroll
            for (int tt = 0; tt < 2; ++tt) sx[tt] = mfma32(T[uu], XF[uu][tt], sx[tt]);
        if (g < 2) { red2[w * 64 + g * 16 + n] = sx[0]; red2[w * 64 + 32 + g * 16 + n] = sx[1]; }
        bar();
        if (w == 1) {
            f4 v = (red2[lane] + red2[64 + lane]) + (red2[128 + lane] + red2[192 + lane]);
            const int tt = lane >> 5, gg = (lane >> 4) & 1, t = lane & 15;
            st16c(rs, O_SXM + (unsigned)((s * 16 + h) * 5 + cj) * KB + (unsigned)((tt * 16 + t) * 8 + 4 * gg) * 4u, __builtin_bit_cast(u4, v));
            vm0();
            if (lane == 0) st4c(rs, FL_A + s * 512 + ci * 4, TAG);
        }
    }
}

DEV void role_X(const Params& P, rsrc_t rs, unsigned char* lds, int xi, unsigned par2, int w, int lane) {
    const int n = lane & 15, g = lane >> 4;
    const int par = w >> 1, tt = w & 1, h = 2 * xi + par;
    const int k0 = w < 2 ? 4 * w : 8 + 3 * (w - 2), nk = w < 2 ? 4 : 3;
    u4* memL = (u4*)lds;
    bool alive = true;
    for (int s = 0; s < NSEG - 1; ++s) {
        if (alive) alive = wait_flags(rs, FL_G + s * 128, 28, lane);
        u4 G[14];
#pragma unroll
        for (int u = 0; u < 14; ++u) G[u] = ld16c(rs, O_GF + (unsigned)(((s * 16 + h) * 2 + tt) * 14 + u) * KB + lane * 16);
        u4 va, vb;
        if (alive) alive = poll_mem(rs, exoff(s, 0, par2), k0, nk, n, g, va, vb);
        if (w == 0) TS(blockIdx.x, s * 8 + 0);
        u4* mL = memL + (s & 1) * 448;
        mem_to_lds(mL, k0, nk, n, g, va, vb);
        bar();
        f4 a = {0, 0, 0, 0};
#pragma unroll
        for (int u = 0; u < 14; ++u) a = mfma32(G[u], mL[(4 * u + g) * 8 + (n & 7)], a);
        if (n < 8) {
            u2 o = {nosent(pk2(a[0], a[1])), nosent(pk2(a[2], a[3]))};
            st8c(rs, exoff(s, 1, par2) + (unsigned)xi * 1024u + tt * 512u + (unsigned)(g * 16 + par * 8 + n) * 8u, o);
        }
        if (w == 0) TS(blockIdx.x, s * 8 + 1);
    }
}

DEV void role_B(const Params& P, rsrc_t rs, unsigned char* lds, int j, unsigned par2, int w, int lane) {
    const int n = lane & 15, g = lane >> 4;
    const int k0 = w < 2 ? 4 * w : 8 + 3 * (w - 2), nk = w < 2 ? 4 : 3;
    u4* memL = (u4*)lds;
    f4* red = (f4*)(lds + 16384);
    u4 BW[4][14];
#pragma unroll
    for (int hh = 0; hh < 4; ++hh)
#pragma unroll
        for (int k = 0; k < 14; ++k) BW[hh][k] = ld16p(rs, O_BF + (unsigned)(((4 * w + hh) * 28 + j) * 14 + k) * KB + lane * 16);
    u4 SEL, SEL2;
    {
        unsigned e[4] = {0, 0, 0, 0};
        if ((n >> 3) == (g & 1)) e[(n & 7) >> 1] = (n & 1) ? 0x3F800000u : 0x00003F80u;
        SEL = u4{e[0], e[1], e[2], e[3]};
        SEL2 = g < 2 ? SEL : u4{0, 0, 0, 0};
    }
    const int di = (n & 7) - 4 * (g & 1);
    const unsigned m01 = (di == 0 ? 0xFFFFu : 0u) | (di == 1 ? 0xFFFF0000u : 0u);
    const unsigned m23 = (di == 2 ? 0xFFFFu : 0u) | (di == 3 ? 0xFFFF0000u : 0u);
    bool alive = true;
    for (int s = 0; s < NSEG; ++s) {
        const bool last = s == NSEG - 1;
        u4 V[4];
        if (!last) {
            if (alive) alive = wait_flags(rs, FL_V + s * 128, 28, lane);
#pragma unroll
            for (int hh = 0; hh < 4; ++hh) V[hh] = ld16c(rs, O_VF + (unsigned)((s * 16 + 4 * w + hh) * 28 + j) * KB + lane * 16);
        }
        u4 va, vb;
        if (alive) alive = poll_mem(rs, exoff(s, 0, par2), k0, nk, n, g, va, vb);
        if (w == 0) TS(blockIdx.x, s * 8 + 0);
        u4* mL = memL + (s & 1) * 448;
        mem_to_lds(mL, k0, nk, n, g, va, vb);
        bar();
        u2 K[4];
        {
            f4 t2[4];
#pragma unroll
            for (int hh = 0; hh < 4; ++hh) t2[hh] = f4{0, 0, 0, 0};
#pragma unroll
            for (int k = 0; k < 14; ++k) {
                const u4 mf = mL[(4 * k + g) * 8 + (n & 7)];
#pragma unroll
                for (int hh = 0; hh < 4; ++hh) t2[hh] = mfma32(mf, BW[hh][k], t2[hh]);
            }
#pragma unroll
            for (int hh = 0; hh < 4; ++hh) K[hh] = u2{pk2(t2[hh][0], t2[hh][1]), pk2(t2[hh][2], t2[hh][3])};
        }
        u2 T2s[4] = {K[0], K[1], K[2], K[3]};
        if (w == 0) TS(blockIdx.x, s * 8 + 1);
        if (!last) {
            u2 sx[2][2];
            {
                const unsigned base = exoff(s, 1, par2) + (unsigned)(2 * w) * 1024u + lane * 8u;
                unsigned it = 0;
                for (; it < SPIN_CAP && alive; ++it) {
                    asm volatile("" ::: "memory");
                    sx[0][0] = ld8c(rs, base); sx[0][1] = ld8c(rs, base + 512); sx[1][0] = ld8c(rs, base + 1024); sx[1][1] = ld8c(rs, base + 1536);
                    if (__all(ok2(sx[0][0]) && ok2(sx[0][1]) && ok2(sx[1][0]) && ok2(sx[1][1]))) break;
                }
                if (it >= SPIN_CAP) alive = false;
            }
            if (w == 0) TS(blockIdx.x, s * 8 + 2);
            float mx[2], lx[2];
#pragma unroll
            for (int p = 0; p < 2; ++p) {
                float v[8] = {bflo(sx[p][0].x), bfhi(sx[p][0].x), bflo(sx[p][0].y), bfhi(sx[p][0].y),
                              bflo(sx[p][1].x), bfhi(sx[p][1].x), bflo(sx[p][1].y), bfhi(sx[p][1].y)};
                float m = fmaxf(fmaxf(fmaxf(v[0], v[1]), fmaxf(v[2], v[3])), fmaxf(fmaxf(v[4], v[5]), fmaxf(v[6], v[7])));
                m = x32max(x16max(m));
                float e[8], l = 0.f;
#pragma unroll
                for (int q = 0; q < 8; ++q) { e[q] = ex2(v[q] - m); l += e[q]; }
                l = x32add(x16add(l));
                mx[p] = m; lx[p] = l;
                const u4 Pm = {pk2(e[0], e[1]), pk2(e[2], e[3]), pk2(e[4], e[5]), pk2(e[6], e[7])};
                const f4 z = {0, 0, 0, 0};
                const f4 oa = mfma32(Pm, V[2 * p], z), ob = mfma32(Pm, V[2 * p + 1], z);
                const u2 oap = {pk2(oa[0], oa[1]), pk2(oa[2], oa[3])}, obp = {pk2(ob[0], ob[1]), pk2(ob[2], ob[3])};
                auto r0 = __builtin_amdgcn_permlane32_swap(K[2 * p].x, oap.x, false, false);
                auto r1 = __builtin_amdgcn_permlane32_swap(K[2 * p].y, oap.y, false, false);
                K[2 * p] = u2{r0[0], r1[0]};
                K[2 * p + 1] = g < 2 ? K[2 * p + 1] : obp;
            }
            const float mxc = g < 2 ? mx[0] : mx[1], lxc = g < 2 ? lx[0] : lx[1];
            if (w == 0) TS(blockIdx.x, s * 8 + 3);
            f4 S;
            {
                unsigned off[3];
#pragma unroll
                for (int q = 0; q < 3; ++q) {
                    const int cj = q < 2 ? 2 * q + (g >> 1) : 4;
                    const int e = (4 * w + 2 * (g & 1) + (n >> 3)) * 5 + cj;
                    off[q] = exoff(s, 2 + (e >= 40), par2) + (unsigned)(e >= 40 ? e - 40 : e) * 128u + (n & 7) * 16u;
                }
                unsigned it = 0;
                for (; it < SPIN_CAP; ++it) {
                    asm volatile("" ::: "memory");
                    const u4 p0 = ld16c(rs, off[0]), p1 = ld16c(rs, off[1]), p2 = ld16c(rs, off[2]);
                    S = mfma32(SEL, p0, f4{0, 0, 0, 0});
                    S = mfma32(SEL, p1, S);
                    S = mfma32(SEL2, p2, S);
                    if (__all(S[0] == S[0]) || !alive) break;
                }
                if (it >= SPIN_CAP) alive = false;
            }
            if (w == 0) TS(blockIdx.x, s * 8 + 4);
            float mn = fmaxf(fmaxf(S[0], S[1]), fmaxf(S[2], S[3]));
            mn = fmaxf(x16max(mn), mxc);
            const float e0 = ex2(S[0] - mn), e1 = ex2(S[1] - mn), e2 = ex2(S[2] - mn), e3 = ex2(S[3] - mn), al = ex2(mxc - mn);
            const float l = lxc * al + x16add((e0 + e1) + (e2 + e3));
            const float ri = __builtin_amdgcn_rcpf(l);
            const unsigned p01 = pk2(e0 * ri, e1 * ri), p23 = pk2(e2 * ri, e3 * ri);
            const float c = al * ri;
            const unsigned cc = pk2(c, c);
            auto a01 = __builtin_amdgcn_permlane32_swap(p01, cc & m01, false, false);
            auto a23 = __builtin_amdgcn_permlane32_swap(p23, cc & m23, false, false);
            const u2 A1 = {a01[0], a23[0]}, A2 = {a01[1], a23[1]};
            const f4 z = {0, 0, 0, 0};
            f4 de = mfma16(K[0], A1, z), dd = mfma16(K[1], A1, z);
            de = mfma16(K[2], A2, de); dd = mfma16(K[3], A2, dd);
            f4 r;
#pragma unroll
            for (int q = 0; q < 4; ++q) r[q] = de[q] + fu(ror8(uf(dd[q])));
            if (n < 8) red[w * 32 + g * 8 + n] = r;
            bar();
            if (w == 0 && lane < 32) {
                const f4 v = (red[lane] + red[32 + lane]) + (red[64 + lane] + red[96 + lane]);
                const u2 o = {nosent(pk2(v[0], v[1])), nosent(pk2(v[2], v[3]))};
                const int gg = lane >> 3, i = lane & 7;
                st8c(rs, exoff(s + 1, 0, par2) + (unsigned)((2 * j + (gg >> 1)) * 8 + i) * 16u + (gg & 1) * 8u, o);
                TS(blockIdx.x, s * 8 + 5);
            }
        }
        if (g < 2) {
#pragma unroll
            for (int hh = 0; hh < 4; ++hh)
                st8c(rs, O_T2F + (unsigned)((s * 28 + j) * 16 + 4 * w + hh) * 256u + n * 16u + g * 8u, T2s[hh]);
        }
        vm0();
        bar();
        if (w == 0 && lane == 0) st4c(rs, FL_B + s * 128 + j * 4, TAG);
    }
}

template <bool ISG>
DEV void role_GV(const Params& P, rsrc_t rs, unsigned char* lds, int wi, int w, int lane) {
    const int n = lane & 15, g = lane >> 4;
    u4* xL = (u4*)lds;
    u4 W[4][14];
#pragma unroll
    for (int c = 0; c < 4; ++c)
#pragma unroll
        for (int k = 0; k < 14; ++k) {
            unsigned off;
            if (ISG) { const int unit = 8 * wi + 2 * w + (c >> 1); off = O_AF + (unsigned)((unit * 2 + (c & 1)) * 14 + k) * KB; }
            else { const int tile = 16 * wi + 4 * w + c; off = O_BF + (unsigned)(tile * 14 + k) * KB; }
            W[c][k] = ld16p(rs, off + lane * 16);
        }
    u4 xr[7];
#pragma unroll
    for (int i = 0; i < 7; ++i) { const int fr = w * 7 + i, tt = fr / 14, k = fr % 14; xr[i] = ldx16(P.x + (size_t)(16 * tt + n) * DM + 32 * k + 8 * g); }
#pragma unroll
    for (int i = 0; i < 7; ++i) xL[(w * 7 + i) * 64 + lane] = xr[i];
    bar();
    for (int s = 0; s < NSEG; ++s) {
        if (s + 1 < NSEG) {
#pragma unroll
            for (int i = 0; i < 7; ++i) { const int fr = w * 7 + i, tt = fr / 14, k = fr % 14; xr[i] = ldx16(P.x + (size_t)(32 * (s + 1) + 16 * tt + n) * DM + 32 * k + 8 * g); }
        }
        const u4* xb = xL + (s & 1) * 28 * 64;
        f4 acc[4][2];
#pragma unroll
        for (int c = 0; c < 4; ++c) { acc[c][0] = f4{0, 0, 0, 0}; acc[c][1] = f4{0, 0, 0, 0}; }
#pragma unroll
        for (int k = 0; k < 14; ++k) {
            const u4 x0 = xb[k * 64 + lane], x1 = xb[(14 + k) * 64 + lane];
#pragma unroll
            for (int c = 0; c < 4; ++c) {
                if (ISG) { acc[c][0] = mfma32(W[c][k], x0, acc[c][0]); acc[c][1] = mfma32(W[c][k], x1, acc[c][1]); }
                else { acc[c][0] = mfma32(x0, W[c][k], acc[c][0]); acc[c][1] = mfma32(x1, W[c][k], acc[c][1]); }
            }
        }
        vm0();
        if (s + 1 < NSEG) {
            u4* xn = xL + ((s + 1) & 1) * 28 * 64;
#pragma unroll
            for (int i = 0; i < 7; ++i) xn[(w * 7 + i) * 64 + lane] = xr[i];
        }
        if (ISG) {
#pragma unroll
            for (int uu = 0; uu < 2; ++uu)
#pragma unroll
                for (int tt = 0; tt < 2; ++tt) {
                    const f4 a = acc[2 * uu][tt], b = acc[2 * uu + 1][tt];
                    const u4 v = {pk2(a[0], a[1]), pk2(a[2], a[3]), pk2(b[0], b[1]), pk2(b[2], b[3])};
                    const int unit = 8 * wi + 2 * w + uu, h = unit / 14, u = unit % 14;
                    st16c(rs, O_GF + (unsigned)(((s * 16 + h) * 2 + tt) * 14 + u) * KB + lane * 16, v);
                }
        } else {
#pragma unroll
            for (int c = 0; c < 4; ++c) {
                const f4 a = acc[c][0], b = acc[c][1];
                const u4 v = {pk2(a[0], a[1]), pk2(a[2], a[3]), pk2(b[0], b[1]), pk2(b[2], b[3])};
                st16c(rs, O_VF + (unsigned)(s * 448 + 16 * wi + 4 * w + c) * KB + lane * 16, v);
            }
        }
        if (s == 0 || s == NSEG - 1) vm0();
        bar();
        if (w == 0) TS(blockIdx.x, s);
        if (w == 0 && lane == 0) {
            const unsigned fb = (ISG ? FL_G : FL_V) + wi * 4;
            if (s == 0 || s == NSEG - 1) st4c(rs, fb + s * 128, TAG);
            if (s >= 2) st4c(rs, fb + (s - 1) * 128, TAG);
        }
    }
}

DEV void task_fft1(const Params& P, rsrc_t rs, int id, int w, int lane) {
    const int n = lane & 15, g = lane >> 4;
    const int ktg = id >> 4, tr = id & 15, kt = 4 * ktg + w;
    if (kt < 15) {
        u4 F[2][2][14];
#pragma unroll
        for (int ri = 0; ri < 2; ++ri)
#pragma unroll
            for (int hl = 0; hl < 2; ++hl)
#pragma unroll
                for (int ks = 0; ks < 14; ++ks) F[ri][hl][ks] = ld16p(rs, O_FW + (unsigned)(((kt * 2 + ri) * 2 + hl) * 14 + ks) * KB + lane * 16);
        for (int tile = 0; tile < 8; ++tile) {
            const int t0 = tr * 128 + tile * 16;
            u4 X[14];
#pragma unroll
            for (int ks = 0; ks < 14; ++ks) X[ks] = ldx16(P.x + (size_t)(t0 + n) * DM + 32 * ks + 8 * g);
            f4 re = {0, 0, 0, 0}, im = {0, 0, 0, 0};
#pragma unroll
            for (int ks = 0; ks < 14; ++ks) {
                re = mfma32(X[ks], F[0][0][ks], re); im = mfma32(X[ks], F[1][0][ks], im);
                re = mfma32(X[ks], F[0][1][ks], re); im = mfma32(X[ks], F[1][1][ks], im);
            }
            const unsigned off = O_Y1 + (unsigned)((16 * kt + n) * 2048 + t0 + 4 * g) * 4u;
            st16c(rs, off, __builtin_bit_cast(u4, re));
            st16c(rs, off + 240 * 2048 * 4, __builtin_bit_cast(u4, im));
        }
    }
    vm0();
    bar();
    if (w == 0 && lane == 0) st4c(rs, FL_1 + id * 4, TAG);
}

DEV int zp(int i) { return i + (i >> 3); }
constexpr int ZC = 2048 + 256;
DEV f2 cmul(f2 a, f2 b) { return f2{a.x * b.x - a.y * b.y, a.x * b.y + a.y * b.x}; }
DEV f2 cmulc(f2 a, f2 b) { return f2{a.x * b.x + a.y * b.y, a.y * b.x - a.x * b.y}; }
template <int R, int S0, bool INV>
DEV void fft_pass(f2* Z, const f2* TW, int tid) {
    constexpr int NE = 1 << R;
    constexpr int LGS = 11 - S0 - R;
    constexpr int STR = 1 << LGS;
    constexpr int NG = 2048 / NE;
    for (int gi = tid; gi < NG; gi += 256) {
        const int lo = gi & (STR - 1), hi = gi >> LGS;
        const int base = (hi << (LGS + R)) + lo;
        f2 tw[R][NE / 2];
#pragma unroll
        for (int q = 0; q < R; ++q) {
            const int half = NE >> (q + 1);
#pragma unroll
            for (int jj = 0; jj < half; ++jj) {
                const int p = lo + jj * STR;
                tw[q][jj] = TW[p << (S0 + q)];
            }
        }
#pragma unroll
        for (int col = 0; col < 4; ++col) {
            f2* z = Z + col * ZC;
            f2 v[NE];
#pragma unroll
            for (int j = 0; j < NE; ++j) v[j] = z[zp(base + j * STR)];
            if (!INV) {
#pragma unroll
                for (int q = 0; q < R; ++q) {
                    const int half = NE >> (q + 1);
#pragma unroll
                    for (int blk = 0; blk < NE; blk += 2 * half)
#pragma unroll
                        for (int jj = 0; jj < half; ++jj) {
                            const f2 a = v[blk + jj], b = v[blk + jj + half];
                            v[blk + jj] = a + b;
                            v[blk + jj + half] = cmul(a - b, tw[q][jj]);
                        }
                }
            } else {
#pragma unroll
                for (int q = R - 1; q >= 0; --q) {
                    const int half = NE >> (q + 1);
#pragma unroll
                    for (int blk = 0; blk < NE; blk += 2 * half)
#pragma unroll
                        for (int jj = 0; jj < half; ++jj) {
                            const f2 a = v[blk + jj], b = cmulc(v[blk + jj + half], tw[q][jj]);
                            v[blk + jj] = a + b;
                            v[blk + jj + half] = a - b;
                        }
                }
            }
#pragma unroll
            for (int j = 0; j < NE; ++j) z[zp(base + j * STR)] = v[j];
        }
    }
}
DEV void task_fft2(const Params& P, rsrc_t rs, unsigned char* lds, int c, int tid, int w, int lane, bool& alive) {
    f2* Z = (f2*)lds;
    f2* TW = Z + 4 * ZC;
    for (int i = tid; i < 1024; i += 256) { float sv, cv; sincospif((float)i * (1.0f / 1024.0f), &sv, &cv); TW[i] = f2{cv, -sv}; }
    if (w == 0 && alive) alive = wait_flags(rs, FL_1 + (c >> 4) * 64, 16, lane);
    bar();
    for (int col = 0; col < 4; ++col) {
        const int k = 4 * c + col;
#pragma unroll
        for (int i = 0; i < 2; ++i) {
            const int t = (tid + 256 * i) * 4;
            const u4 re = ld16c(rs, O_Y1 + (unsigned)(k * 2048 + t) * 4u), im = ld16c(rs, O_Y1 + (unsigned)((240 + k) * 2048 + t) * 4u);
            f2* z = Z + col * ZC;
            z[zp(t + 0)] = f2{fu(re.x), fu(im.x)}; z[zp(t + 1)] = f2{fu(re.y), fu(im.y)};
            z[zp(t + 2)] = f2{fu(re.z), fu(im.z)}; z[zp(t + 3)] = f2{fu(re.w), fu(im.w)};
        }
    }
    bar();
    fft_pass<3, 0, false>(Z, TW, tid); bar();
    fft_pass<3, 3, false>(Z, TW, tid); bar();
    fft_pass<3, 6, false>(Z, TW, tid); bar();
    fft_pass<2, 9, false>(Z, TW, tid); bar();
    for (int i = 0; i < 8; ++i) {
        const int p = tid + 256 * i;
        const u4 wv = ld16p(rs, O_WT + (unsigned)(c * 2048 + p) * 16u);
        const unsigned ww[4] = {wv.x, wv.y, wv.z, wv.w};
#pragma unroll
        for (int col = 0; col < 4; ++col) {
            f2* z = Z + col * ZC + zp(p);
            *z = cmul(*z, f2{bflo(ww[col]), bfhi(ww[col])});
        }
    }
    bar();
    fft_pass<2, 9, true>(Z, TW, tid); bar();
    fft_pass<3, 6, true>(Z, TW, tid); bar();
    fft_pass<3, 3, true>(Z, TW, tid); bar();
    fft_pass<3, 0, true>(Z, TW, tid); bar();
    for (int i = 0; i < 8; ++i) {
        const int t = tid + 256 * i;
        unsigned hi[4], lo[4];
#pragma unroll
        for (int col = 0; col < 4; ++col) {
            const f2 v = Z[col * ZC + zp(t)];
            hi[col] = pk2(v.x, v.y);
            lo[col] = pk2(v.x - bflo(hi[col]), v.y - bfhi(hi[col]));
        }
        const unsigned off = O_Y2 + (unsigned)(((t >> 4) * 15 + (c >> 2)) * 2) * KB + (unsigned)((c & 3) * 16 + (t & 15)) * 16u;
        st16c(rs, off, u4{hi[0], hi[1], hi[2], hi[3]});
        st16c(rs, off + KB, u4{lo[0], lo[1], lo[2], lo[3]});
    }
    vm0();
    bar();
    if (w == 0 && lane == 0) st4c(rs, FL_2 + c * 4, TAG);
}

DEV void task_x1(const Params& P, rsrc_t rs, int s, int q, int w, int lane, bool& alive) {
    const int n = lane & 15, g = lane >> 4, h = 4 * q + w;
    if (alive) alive = wait_flags(rs, FL_G + s * 128, 28, lane);
    f4 a00 = {0, 0, 0, 0}, a10 = {0, 0, 0, 0}, a11 = {0, 0, 0, 0};
#pragma unroll
    for (int u = 0; u < 14; ++u) {
        const u4 g0 = ld16c(rs, O_GF + (unsigned)(((s * 16 + h) * 2 + 0) * 14 + u) * KB + lane * 16);
        const u4 g1 = ld16c(rs, O_GF + (unsigned)(((s * 16 + h) * 2 + 1) * 14 + u) * KB + lane * 16);
        const u4 x0 = ldx16(P.x + (size_t)(32 * s + n) * DM + 32 * u + 8 * g);
        const u4 x1 = ldx16(P.x + (size_t)(32 * s + 16 + n) * DM + 32 * u + 8 * g);
        a00 = mfma32(g0, x0, a00); a10 = mfma32(g0, x1, a10); a11 = mfma32(g1, x1, a11);
    }
    const unsigned off = O_SXX + (unsigned)((s * 16 + h) * 2) * 2048u + lane * 32u;
    const u4 z = {0, 0, 0, 0};
    st16c(rs, off, __builtin_bit_cast(u4, a00)); st16c(rs, off + 16, z);
    st16c(rs, off + 2048, __builtin_bit_cast(u4, a10)); st16c(rs, off + 2048 + 16, __builtin_bit_cast(u4, a11));
    vm0();
    bar();
    if (w == 0 && lane == 0) st4c(rs, FL_X1 + s * 128 + q * 4, TAG);
}

DEV void allg(float v, float o[4]) {
    auto r = __builtin_amdgcn_permlane16_swap(uf(v), uf(v), false, false);
    auto a = __builtin_amdgcn_permlane32_swap(r[0], r[0], false, false);
    auto b = __builtin_amdgcn_permlane32_swap(r[1], r[1], false, false);
    o[0] = fu(a[0]); o[2] = fu(a[1]); o[1] = fu(b[0]); o[3] = fu(b[1]);
}

DEV void task_late(const Params& P, rsrc_t rs, unsigned char* lds, int s, int q, int tid, int w, int lane, bool& alive) {
    const int n = lane & 15, g = lane >> 4;
    u4* YL = (u4*)lds;
    float* OB = (float*)(lds + 61440);
    float* OA = (float*)lds;
    if (w == 0 && alive) alive = wait_flags(rs, FL_2, 57, lane);
    bar();
    for (int i = tid; i < 60 * 64; i += 256) YL[i] = ld16c(rs, O_Y2 + (unsigned)(2 * s) * 30u * KB + i * 16u);
    bar();
    {
        for (int fl = w; fl < 7; fl += 4) {
            const int ft = 7 * q + fl;
            f4 acc[2] = {f4{0, 0, 0, 0}, f4{0, 0, 0, 0}};
#pragma unroll 5
            for (int u = 0; u < 15; ++u) {
                const u4 fh = ld16p(rs, O_FI + (unsigned)((ft * 2 + 0) * 15 + u) * KB + lane * 16), fl2 = ld16p(rs, O_FI + (unsigned)((ft * 2 + 1) * 15 + u) * KB + lane * 16);
#pragma unroll
                for (int tt = 0; tt < 2; ++tt) {
                    const u4 yh = YL[((tt * 15 + u) * 2 + 0) * 64 + lane], yl = YL[((tt * 15 + u) * 2 + 1) * 64 + lane];
                    acc[tt] = mfma32(fh, yh, acc[tt]); acc[tt] = mfma32(fl2, yh, acc[tt]); acc[tt] = mfma32(fh, yl, acc[tt]);
                }
            }
#pragma unroll
            for (int tt = 0; tt < 2; ++tt) *(f4*)(OB + (16 * tt + n) * 112 + 16 * fl + 4 * g) = acc[tt];
        }
    }
    if (alive) alive = wait_flags(rs, FL_V + s * 128, 28, lane);
    u4 V[4][7];
#pragma unroll
    for (int hh = 0; hh < 4; ++hh)
#pragma unroll
        for (int fl = 0; fl < 7; ++fl) V[hh][fl] = ld16c(rs, O_VF + (unsigned)((s * 16 + 4 * w + hh) * 28 + 7 * q + fl) * KB + lane * 16);
    if (alive) alive = wait_flags(rs, FL_X1 + s * 128, 4, lane);
    f4 SX[4][2][2];
#pragma unroll
    for (int hh = 0; hh < 4; ++hh)
#pragma unroll
        for (int tt = 0; tt < 2; ++tt) {
            const unsigned off = O_SXX + (unsigned)((s * 16 + 4 * w + hh) * 2 + tt) * 2048u + lane * 32u;
            SX[hh][tt][0] = __builtin_bit_cast(f4, ld16c(rs, off)); SX[hh][tt][1] = __builtin_bit_cast(f4, ld16c(rs, off + 16));
        }
    if (alive) alive = wait_flags(rs, FL_A + s * 512, 80, lane);
    f4 SM[2][2];
#pragma unroll
    for (int tt = 0; tt < 2; ++tt) {
        f4 a = {0, 0, 0, 0}, b = {0, 0, 0, 0};
#pragma unroll
        for (int cj = 0; cj < 5; ++cj) {
            const unsigned off = O_SXM + (unsigned)((s * 16 + 4 * w + g) * 5 + cj) * KB + (unsigned)((tt * 16 + n) * 8) * 4u;
            a += __builtin_bit_cast(f4, ld16c(rs, off)); b += __builtin_bit_cast(f4, ld16c(rs, off + 16));
        }
        SM[tt][0] = a; SM[tt][1] = b;
    }
    if (alive) alive = wait_flags(rs, FL_B + s * 128 + 7 * q * 4, 7, lane);
    u4 T2[7];
#pragma unroll
    for (int fl = 0; fl < 7; ++fl) T2[fl] = ld16c(rs, O_T2F + (unsigned)((s * 28 + 7 * q + fl) * 16 + 4 * w + g) * 256u + n * 16u);
    u4 PX[4][2], PM[2];
#pragma unroll
    for (int tt = 0; tt < 2; ++tt) {
        float mm = fmaxf(fmaxf(fmaxf(SM[tt][0][0], SM[tt][0][1]), fmaxf(SM[tt][0][2], SM[tt][0][3])), fmaxf(fmaxf(SM[tt][1][0], SM[tt][1][1]), fmaxf(SM[tt][1][2], SM[tt][1][3])));
        float mmb[4]; allg(mm, mmb);
        float m[4], lx[4], e[4][8];
#pragma unroll
        for (int hh = 0; hh < 4; ++hh) {
            float v[8];
#pragma unroll
            for (int jj = 0; jj < 8; ++jj) {
                const int tp = 16 * (jj >> 2) + 4 * g + (jj & 3);
                v[jj] = tp <= 16 * tt + n ? SX[hh][tt][jj >> 2][jj & 3] : -3.0e38f;
            }
            float mx = fmaxf(fmaxf(fmaxf(v[0], v[1]), fmaxf(v[2], v[3])), fmaxf(fmaxf(v[4], v[5]), fmaxf(v[6], v[7])));
            mx = fmaxf(x32max(x16max(mx)), mmb[hh]);
            float l = 0.f;
#pragma unroll
            for (int jj = 0; jj < 8; ++jj) { e[hh][jj] = ex2(v[jj] - mx); l += e[hh][jj]; }
            m[hh] = mx; lx[hh] = x32add(x16add(l));
        }
        const float mo = g == 0 ? m[0] : (g == 1 ? m[1] : (g == 2 ? m[2] : m[3]));
        float em[8], lm = 0.f;
#pragma unroll
        for (int jj = 0; jj < 8; ++jj) { em[jj] = ex2(SM[tt][jj >> 2][jj & 3] - mo); lm += em[jj]; }
        float lmb[4]; allg(lm, lmb);
        float ri[4];
#pragma unroll
        for (int hh = 0; hh < 4; ++hh) {
            ri[hh] = 1.0f / (lx[hh] + lmb[hh]);
            PX[hh][tt] = u4{pk2(e[hh][0] * ri[hh], e[hh][1] * ri[hh]), pk2(e[hh][2] * ri[hh], e[hh][3] * ri[hh]),
                            pk2(e[hh][4] * ri[hh], e[hh][5] * ri[hh]), pk2(e[hh][6] * ri[hh], e[hh][7] * ri[hh])};
        }
        const float ro = g == 0 ? ri[0] : (g == 1 ? ri[1] : (g == 2 ? ri[2] : ri[3]));
        PM[tt] = u4{pk2(em[0] * ro, em[1] * ro), pk2(em[2] * ro, em[3] * ro), pk2(em[4] * ro, em[5] * ro), pk2(em[6] * ro, em[7] * ro)};
    }
    bar();
#pragma unroll
    for (int fl = 0; fl < 7; ++fl)
#pragma unroll
        for (int tt = 0; tt < 2; ++tt) {
            f4 a = mfma32(T2[fl], PM[tt], f4{0, 0, 0, 0});
#pragma unroll
            for (int hh = 0; hh < 4; ++hh) a = mfma32(V[hh][fl], PX[hh][tt], a);
            *(f4*)(OA + (w * 32 + 16 * tt + n) * 112 + 16 * fl + 4 * g) = a;
        }
    bar();
    for (int i = tid; i < 32 * 14; i += 256) {
        const int t = i / 14, ch = i % 14;
        const float gt = *(const float*)(P.ws + O_GATE + (32 * s + t) * 4);
        unsigned o[4];
#pragma unroll
        for (int e2 = 0; e2 < 4; ++e2) {
            float r[2];
#pragma unroll
            for (int z = 0; z < 2; ++z) {
                const int f = ch * 8 + 2 * e2 + z, ix = t * 112 + f;
                const float a = (OA[ix] + OA[3584 + ix]) + (OA[2 * 3584 + ix] + OA[3 * 3584 + ix]);
                const float ab = bflo(pk2(a, 0.f));
                const float bb = bflo(pk2(OB[ix] * (1.0f / (2048.0f * 448.0f)), 0.f));
                r[z] = gt * ab + (1.0f - gt) * bb;
            }
            o[e2] = pk2(r[0], r[1]);
        }
        *(u4*)(P.out + (size_t)(32 * s + t) * DM + 112 * q + ch * 8) = u4{o[0], o[1], o[2], o[3]};
    }
    bar();
}

DEV void role_general(const Params& P, rsrc_t rs, unsigned char* lds, int wk, int tid, int w, int lane) {
    bool alive = true;
    if (w == 0) TS(blockIdx.x, 0);
    if (wk < 64) task_fft1(P, rs, wk, w, lane);
    if (w == 0) TS(blockIdx.x, 1);
    if (wk < 57) task_fft2(P, rs, lds, wk, tid, w, lane, alive);
    if (w == 0) TS(blockIdx.x, 2);
    for (int id = wk; id < 512; id += NGW) {
        const int s = id >> 3, r = id & 7;
        if (r < 4) task_x1(P, rs, s, r, w, lane, alive);
        else task_late(P, rs, lds, s, r - 4, tid, w, lane, alive);
        if (w == 0) TS(blockIdx.x, 3 + id / NGW);
    }
}

__global__ void __launch_bounds__(256, 1) main_kernel(Params P) {
    extern __shared__ __attribute__((aligned(16))) unsigned char lds[];
    const int b = blockIdx.x, tid = threadIdx.x, lane = tid & 63, w = __builtin_amdgcn_readfirstlane(tid >> 6);
    rsrc_t rs = __builtin_amdgcn_make_buffer_rsrc(P.ws, 0, 0x10000000, 0x00020000);
    const unsigned par2 = (*(const unsigned*)(P.ws + O_PAR) & 1u) | ((*(const unsigned*)(P.ws + O_PAR + 4) & 1u) << 1);
    const int xcd = b & 7, slot = b >> 3;
    if (w == 0) TS(b, 4000);
    if (xcd < 4) {
        const int ci = slot * 4 + xcd;
        if (ci < NA) role_A(P, rs, lds, ci, par2, w, lane);
        else if (ci < NA + NX) role_X(P, rs, lds, ci - NA, par2, w, lane);
        else if (ci < NA + NX + NB) role_B(P, rs, lds, ci - NA - NX, par2, w, lane);
        else role_general(P, rs, lds, 72 + ci - (NA + NX + NB), tid, w, lane);
    } else {
        const int fi = slot * 4 + xcd - 4;
        if (fi < 28) role_GV<true>(P, rs, lds, fi, w, lane);
        else if (fi < NGV) role_GV<false>(P, rs, lds, fi - 28, w, lane);
        else role_general(P, rs, lds, fi - NGV, tid, w, lane);
    }
}

extern "C" void kernel_launch(void* const* d_in, const int* in_sizes, int n_in, void* d_out, int out_size, void* d_ws, size_t ws_size,
                              hipStream_t stream) {
    (void)in_sizes; (void)n_in; (void)out_size; (void)ws_size;
    Params P;
    P.x = (const us*)d_in[0]; P.mem0 = (const us*)d_in[1]; P.wq = (const us*)d_in[2]; P.wk = (const us*)d_in[3];
    P.wv = (const us*)d_in[4]; P.wo = (const us*)d_in[5]; P.wr = (const us*)d_in[8]; P.wi = (const us*)d_in[9];
    P.gw = (const us*)d_in[10]; P.out = (us*)d_out; P.ws = (unsigned char*)d_ws;
    (void)hipFuncSetAttribute((const void*)prep_kernel, hipFuncAttributeMaxDynamicSharedMemorySize, LDS_BYTES);
    (void)hipFuncSetAttribute((const void*)main_kernel, hipFuncAttributeMaxDynamicSharedMemorySize, LDS_BYTES);
    prep_kernel<<<256, 256, LDS_BYTES, stream>>>(P);
    main_kernel<<<256, 256, LDS_BYTES, stream>>>(P);
}
```

```cpp
#include <hip/hip_runtime.h>
#include <hip/hip_bf16.h>
#include <stdint.h>

typedef __attribute__((ext_vector_type(8))) __bf16 bf8;
typedef __attribute__((ext_vector_type(2))) __bf16 bf2;
typedef __attribute__((ext_vector_type(4))) float f4;
typedef __attribute__((ext_vector_type(2))) float f2;
typedef __attribute__((ext_vector_type(4))) unsigned u4;
typedef __attribute__((ext_vector_type(2))) unsigned u2;
typedef __attribute__((ext_vector_type(4))) short s4;
typedef unsigned short us;
typedef __amdgpu_buffer_rsrc_t rsrc_t;
#define DEV __device__ __forceinline__

constexpr int SLEN = 2048, DM = 448, NH = 16, NSEG = 64;
#define NGW_N 76
constexpr int NA = 80, NX = 16, NB = 28, NGV = 56, NGW = NGW_N;
constexpr unsigned KB = 1024;
constexpr unsigned O_AF = 0;
constexpr unsigned O_BF = O_AF + 16 * 14 * 2 * 14 * KB;
constexpr unsigned O_GF = O_BF + 16 * 28 * 14 * KB;
constexpr unsigned O_VF = O_GF + 64 * 16 * 28 * KB;
constexpr unsigned O_SXX = O_VF + 64 * 16 * 28 * KB;
constexpr unsigned O_SXM = O_SXX + 64 * 16 * 4 * KB;
constexpr unsigned O_T2F = O_SXM + 64 * 80 * KB;
constexpr unsigned O_FW = O_T2F + 64 * 28 * 16 * 256;
constexpr unsigned O_FI = O_FW + 15 * 4 * 14 * KB;
constexpr unsigned O_WT = O_FI + 28 * 2 * 15 * KB;
constexpr unsigned O_Y1 = O_WT + 57 * 2048 * 16;
constexpr unsigned O_Y2 = O_Y1 + 2 * 240 * 2048 * 4;
constexpr unsigned O_GATE = O_Y2 + 128 * 15 * 2 * KB;
constexpr unsigned O_XF = O_GATE + 2048 * 4;
constexpr unsigned O_FL = O_XF + 64 * 28 * KB;
constexpr unsigned FL_X1 = O_FL;
constexpr unsigned FL_1 = FL_X1 + 64 * 128;
constexpr unsigned FL_2 = FL_1 + 256;
constexpr unsigned FL_CG = FL_2 + 256;
constexpr unsigned FL_CV = FL_CG + 28 * 256;
constexpr unsigned FL_CA = FL_CV + 28 * 256;
constexpr unsigned FL_CB = FL_CA + 80 * 256;
constexpr unsigned FL_END = FL_CB + 28 * 256;
constexpr unsigned O_PAR = FL_END;
constexpr unsigned O_DBG = O_PAR + 256;
constexpr unsigned O_EX = ((O_DBG + 65536 + (2u << 20) - 1) >> 21) << 21;
constexpr unsigned WS_NEED = O_EX + (4u << 20);
constexpr unsigned SENT = 0xFFFFFFFFu;
constexpr unsigned TAG = 1u;
constexpr unsigned SPIN_CAP = 1u << 17;
constexpr unsigned SPIN_ASM = 1u << 18;
constexpr int LDS_BYTES = 128 * 1024;
constexpr int PREP_LDS = 147456;
#define PJ_P 202
#define PJ_QD 106
#define PJ_QE 100
#define PJ_C0 36
#define PJ_R 80
#define PJ_SW 32
#define PJ_DP 0
#define PJ_XINC 4
#define PJ_XCAP 16
#define PJ_MCAP 12
#define PJ_R1 4
#define PJ_PINC 3
#define PJ_D1 8
#define PJ_D2 4
#define PJ_PCAP 20
#define PJ_PA0 4
#define PJ_C1 44
#define PJ_PINC2 0
#define PJ_DST 0
#define PJ_FST 1
#define PJ_TH 1
constexpr unsigned STAMP_OFF = 32768u + 5120u;

struct Params {
    const us* x; const us* mem0; const us* wq; const us* wk; const us* wv; const us* wo;
    const us* wr; const us* wi; const us* gw; us* out; unsigned char* ws;
};

#define TS(slot, idx) do { } while (0)
DEV unsigned pk2(float lo, float hi) { f2 v = {lo, hi}; bf2 b = __builtin_convertvector(v, bf2); return __builtin_bit_cast(unsigned, b); }
DEV float bflo(unsigned v) { return __builtin_bit_cast(float, v << 16); }
DEV float bfhi(unsigned v) { return __builtin_bit_cast(float, v & 0xFFFF0000u); }
DEV float bf2f(us v) { return __builtin_bit_cast(float, (unsigned)v << 16); }
DEV f4 mfma32(u4 a, u4 b, f4 c) { return __builtin_amdgcn_mfma_f32_16x16x32_bf16(__builtin_bit_cast(bf8, a), __builtin_bit_cast(bf8, b), c, 0, 0, 0); }
DEV f4 mfma16(u2 a, u2 b, f4 c) { return __builtin_amdgcn_mfma_f32_16x16x16bf16_1k(__builtin_bit_cast(s4, a), __builtin_bit_cast(s4, b), c, 0, 0, 0); }
DEV u4 ld16c(rsrc_t r, unsigned off) { return __builtin_amdgcn_raw_buffer_load_b128(r, off, 0, 16); }
DEV u2 ld8c(rsrc_t r, unsigned off) { return __builtin_amdgcn_raw_buffer_load_b64(r, off, 0, 16); }
DEV unsigned ld4c(rsrc_t r, unsigned off) { return __builtin_amdgcn_raw_buffer_load_b32(r, off, 0, 16); }
DEV u4 ld16p(rsrc_t r, unsigned off) { return __builtin_amdgcn_raw_buffer_load_b128(r, off, 0, 0); }
DEV void st16c(rsrc_t r, unsigned off, u4 v) { __builtin_amdgcn_raw_buffer_store_b128(v, r, off, 0, 16); }
DEV void st8c(rsrc_t r, unsigned off, u2 v) { __builtin_amdgcn_raw_buffer_store_b64(v, r, off, 0, 16); }
DEV void st4c(rsrc_t r, unsigned off, unsigned v) { __builtin_amdgcn_raw_buffer_store_b32(v, r, off, 0, 16); }
DEV void vm0() { asm volatile("s_waitcnt vmcnt(0)" ::: "memory"); }
DEV void bar() { __syncthreads(); }
DEV float fu(unsigned v) { return __builtin_bit_cast(float, v); }
DEV unsigned uf(float v) { return __builtin_bit_cast(unsigned, v); }
DEV float ex2(float v) { return __builtin_amdgcn_exp2f(v); }
DEV float x16max(float v) { auto r = __builtin_amdgcn_permlane16_swap(uf(v), uf(v), false, false); return fmaxf(fu(r[0]), fu(r[1])); }
DEV float x32max(float v) { auto r = __builtin_amdgcn_permlane32_swap(uf(v), uf(v), false, false); return fmaxf(fu(r[0]), fu(r[1])); }
DEV float x16add(float v) { auto r = __builtin_amdgcn_permlane16_swap(uf(v), uf(v), false, false); return fu(r[0]) + fu(r[1]); }
DEV float x32add(float v) { auto r = __builtin_amdgcn_permlane32_swap(uf(v), uf(v), false, false); return fu(r[0]) + fu(r[1]); }
DEV unsigned ror8(unsigned v) { return (unsigned)__builtin_amdgcn_update_dpp(0, (int)v, 0x128, 0xf, 0xf, false); }
DEV u4 ror8(u4 v) { u4 r = {ror8(v.x), ror8(v.y), ror8(v.z), ror8(v.w)}; return r; }
DEV unsigned nosent(unsigned v) { return v < 0xFFFFFFFEu ? v : 0xFFFFFFFEu; }
DEV bool ok4(u4 v) { return v.x != SENT && v.y != SENT && v.z != SENT && v.w != SENT; }
DEV bool ok2(u2 v) { return v.x != SENT && v.y != SENT; }
DEV unsigned exoff(int s, int b, unsigned par2) { return O_EX + (unsigned)(s * 4 + b) * 16384u + ((par2 >> (s >> 5)) & 1u) * 8192u; }
DEV u4 ldx16(const us* p) { return *(const u4*)p; }

DEV bool wait_flags(rsrc_t r, unsigned off, int n, int lane) {
    const unsigned o0 = off + 4u * (lane < n ? lane : 0), o1 = off + 4u * (lane + 64 < n ? lane + 64 : 0);
    for (unsigned it = 0; it < SPIN_CAP; ++it) {
        asm volatile("" ::: "memory");
        unsigned a = ld4c(r, o0), b = TAG;
        if (n > 64) b = ld4c(r, o1);
        if (__all(a == TAG && b == TAG)) return true;
        __builtin_amdgcn_s_sleep(1);
    }
    return false;
}

#define PREP_PF 3
#define PREP_ST2(o, v) (*(u4*)(P.ws + (o)) = (v))
#define PREP_ST(o, v) st16c(rs, (o), (v))
#define GV_LAG 1
#define WSLEEP 32
#define GV_EAGER 2
#define GV_LEAD 8
DEV bool wait_cnt(rsrc_t r, unsigned off, int n, unsigned need, int lane, bool near = false) {
    const unsigned o0 = off + 256u * (lane < n ? lane : 0), o1 = off + 256u * (lane + 64 < n ? lane + 64 : 0);
    for (unsigned it = 0; it < SPIN_CAP; ++it) {
        asm volatile("" ::: "memory");
        unsigned a = ld4c(r, o0), b = need;
        if (n > 64) b = ld4c(r, o1);
        if (__all(a >= need && b >= need)) return true;
        const unsigned nd = 1u;
        if (near && __all(a + nd >= need && b + nd >= need)) return true;
        if (__any(a + 1 < need || b + 1 < need)) __builtin_amdgcn_s_sleep(WSLEEP); else
        __builtin_amdgcn_s_sleep(4);
    }
    return false;
}

DEV bool wait_cnt2(rsrc_t r, unsigned offa, unsigned offb, unsigned need, int lane) {
    const unsigned o0 = offa + 256u * lane, o1 = lane < 16 ? offa + 256u * (64 + lane) : offb + 256u * (lane < 23 ? lane - 16 : 0);
    for (unsigned it = 0; it < SPIN_CAP; ++it) {
        asm volatile("" ::: "memory");
        const unsigned a = ld4c(r, o0), b = ld4c(r, o1);
        if (__all(a >= need && b >= need)) return true;
        if (need >= 63) __builtin_amdgcn_s_sleep(2); else __builtin_amdgcn_s_sleep(6);
    }
    return false;
}
#define PSTAMP(i) do { } while (0)
#define GSTAMP(i) do { } while (0)
DEV int pswz(int r) { return (((r >> 3) & 3) << 2) | (r & 3); }
DEV int bitrev11(int p) { return (int)(__builtin_bitreverse32((unsigned)p) >> 21); }

__global__ void __launch_bounds__(256, 1) prep_kernel(Params P) {
    extern __shared__ __attribute__((aligned(16))) unsigned char lds[];
    const int b = blockIdx.x, tid = threadIdx.x, lane = tid & 63, w = __builtin_amdgcn_readfirstlane(tid >> 6);
    const int n = lane & 15, g = lane >> 4;
    const int xcd = b & 7, slot = b >> 3;
    rsrc_t rs = __builtin_amdgcn_make_buffer_rsrc(P.ws, 0, 0x10000000, 0x00020000);
    if (w == 0) TS(b, 4001);
    const bool mm = slot < 28;
    const int grp = xcd * 4 + slot / 7, up = slot % 7, h = grp & 15;
    const bool isA = grp >= 16;
    const us* W1 = isA ? P.wk : P.wv;
    us wrv[2][4], wiv[2][4];
#pragma unroll
    for (int r = 0; r < 2; ++r) {
        const int f = bitrev11(b * 8 + w * 2 + r);
#pragma unroll
        for (int i = 0; i < 4; ++i) {
            const int k = lane + 64 * i, kk = k < 225 ? k : 0;
            wrv[r][i] = P.wr[f * 225 + kk]; wiv[r][i] = P.wi[f * 225 + kk];
        }
    }
    u4 xg[2], gw;
    {
        const int l2 = lane < 56 ? lane : 0;
        gw = ldx16(P.gw + l2 * 8);
#pragma unroll
        for (int r = 0; r < 2; ++r) xg[r] = ldx16(P.x + (b * 8 + w * 2 + r) * DM + l2 * 8);
    }
    __builtin_amdgcn_sched_barrier(0);
    unsigned tch = 0, tch2 = 0;
    if (mm) {
        const int id = up * 256 + tid;
        tch = *(const unsigned*)(W1 + ((size_t)(id >> 2) * NH + h) * 256 + (id & 3) * 64);
        tch2 = isA ? *(const unsigned*)(P.wq + ((size_t)(64 * up + (tid >> 2)) * NH + h) * 256 + (tid & 3) * 64)
                   : *(const unsigned*)(P.wo + ((size_t)h * 256 + tid) * DM + 64 * up);
    }
    __builtin_amdgcn_sched_barrier(0);
    if (w == 0) TS(b, 4002);
    float* ct = (float*)(lds + 98304);
    float* st = ct + 448;
    unsigned* T = (unsigned*)(lds + 98304 + 4096);
    for (int i = tid; i < 448; i += 256) { float sv, cv; sincospif((float)i * (2.0f / 448.0f), &sv, &cv); ct[i] = cv; st[i] = sv; }
    bar();
    {
        const int id = b * 4 + w;
        if (id < 840) {
            float v[8];
            unsigned oh, ol;
            if (id < 420) {
                const int kt = id / 28, ri = (id / 14) & 1, ks = id % 14;
                const int k = 16 * kt + n;
#pragma unroll
                for (int j = 0; j < 8; ++j) {
                    const int d = 32 * ks + 8 * g + j;
                    const int m = (d * k) % 448;
                    v[j] = k <= 224 ? (ri == 0 ? ct[m] : -st[m]) : 0.0f;
                }
                oh = O_FW + (unsigned)(((kt * 2 + ri) * 2 + 0) * 14 + ks) * KB;
                ol = oh + 14 * KB;
            } else {
                const int id2 = id - 420, ft = id2 / 15, u = id2 % 15;
                const int d = 16 * ft + n;
#pragma unroll
                for (int j = 0; j < 8; ++j) {
                    const int k = 4 * (4 * u + g) + (j >> 1);
                    const int m = (d * k) % 448;
                    const float ck = (k == 0 || k == 224) ? 1.0f : 2.0f;
                    v[j] = k <= 224 ? ck * ((j & 1) == 0 ? ct[m] : -st[m]) : 0.0f;
                }
                oh = O_FI + (unsigned)((ft * 2 + 0) * 15 + u) * KB;
                ol = oh + 15 * KB;
            }
            unsigned hh[4], ll[4];
#pragma unroll
            for (int j = 0; j < 4; ++j) {
                hh[j] = pk2(v[2 * j], v[2 * j + 1]);
                ll[j] = pk2(v[2 * j] - bflo(hh[j]), v[2 * j + 1] - bfhi(hh[j]));
            }
            PREP_ST2(oh + lane * 16, (u4{hh[0], hh[1], hh[2], hh[3]}));
            PREP_ST2(ol + lane * 16, (u4{ll[0], ll[1], ll[2], ll[3]}));
        }
    }
#pragma unroll
    for (int r = 0; r < 2; ++r)
#pragma unroll
        for (int i = 0; i < 4; ++i) { const int k = lane + 64 * i; if (k < 228) T[(w * 2 + r) * 232 + k] = k < 225 ? ((unsigned)wrv[r][i] | ((unsigned)wiv[r][i] << 16)) : 0u; }
    bar();
    for (int i = tid; i < 57 * 8; i += 256) {
        const int c = i >> 3, r = i & 7;
        PREP_ST2(O_WT + (unsigned)(c * 2048 + b * 8 + r) * 16u, *(const u4*)(T + r * 232 + 4 * c));
    }
#pragma unroll
    for (int r = 0; r < 2; ++r) {
        const u4 xv = xg[r];
        float s = bflo(xv.x) * bflo(gw.x) + bfhi(xv.x) * bfhi(gw.x) + bflo(xv.y) * bflo(gw.y) + bfhi(xv.y) * bfhi(gw.y)
                + bflo(xv.z) * bflo(gw.z) + bfhi(xv.z) * bfhi(gw.z) + bflo(xv.w) * bflo(gw.w) + bfhi(xv.w) * bfhi(gw.w);
        if (lane < 56) {
            const int t = b * 8 + w * 2 + r;
            PREP_ST2(O_XF + (unsigned)(((t >> 4) * 14 + (lane >> 2)) * 64 + (lane & 3) * 16 + (t & 15)) * 16u, xv);
        }
        if (lane >= 56) s = 0.f;
        for (int o = 32; o > 0; o >>= 1) s += __shfl_xor(s, o);
        if (lane == 0) *(float*)(P.ws + O_GATE + (b * 8 + w * 2 + r) * 4) = 1.0f / (1.0f + __expf(-s));
    }
    if (w == 0) TS(b, 4007);
    __builtin_amdgcn_sched_barrier(0);
    u4 Bsrc[8], Ast[16];
    if (mm) {
        if (isA) {
#pragma unroll
            for (int i = 0; i < 8; ++i) Bsrc[i] = ldx16(P.wq + ((size_t)(64 * up + 16 * w + 2 * i + (lane >> 5)) * NH + h) * 256 + (lane & 31) * 8);
        } else {
#pragma unroll
            for (int i = 0; i < 8; ++i) Bsrc[i] = ldx16(P.wo + ((size_t)h * 256 + 64 * w + 8 * i + (lane >> 3)) * DM + 64 * up + (lane & 7) * 8);
        }
#pragma unroll
        for (int i = 0; i < 16; ++i) Ast[i] = ldx16(W1 + ((size_t)(32 * w + 2 * i + (lane >> 5)) * NH + h) * 256 + (lane & 31) * 8);
    }
    __builtin_amdgcn_sched_barrier(0);
    {
        const u4 sv = {SENT, SENT, SENT, SENT};
        if (xcd >= 4) {
#pragma unroll
            for (int rep = 0; rep < 2; ++rep) {
                const int id = (slot * 4 + xcd - 4) * 2 + rep, s = id >> 2, blk = id & 3;
                const unsigned len = blk == 0 ? 7168u : (blk == 1 ? 8192u : (blk == 2 ? 5632u : 7168u));
                const unsigned base = O_EX + (unsigned)(s * 4 + blk) * 16384u;
#pragma unroll
                for (int i = 0; i < 2; ++i) {
                    const unsigned o = tid * 16u + i * 4096u;
                    const bool z0 = s == 0 && blk == 2 && o >= 5120u;
                    const u4 v = z0 ? u4{0, 0, 0, 0} : sv;
                    if (o < len && id != 0) { PREP_ST2(base + o, v); PREP_ST2(base + 8192u + o, v); }
                }
            }
        } else if (b == 251) {
#pragma unroll
            for (int i = 0; i < 2; ++i) {
                const unsigned o = tid * 16u + i * 4096u;
                if (o < 7168u) { const int ch = o >> 4; const u4 v = ldx16(P.mem0 + (ch & 7) * DM + (ch >> 3) * 8); st16c(rs, O_EX + o, v); st16c(rs, O_EX + 8192u + o, v); }
            }
        }
        const unsigned nfl = (FL_END - O_FL) / 16;
        const u4 z = {0, 0, 0, 0};
        for (unsigned i = b * 256 + tid; i < nfl; i += 65536) *(u4*)(P.ws + O_FL + i * 16) = z;
        if (tid < 48) { const int i = b * 48 + tid, tb = i / 96, hl = (i / 48) & 1, e = i % 48; *(u4*)(P.ws + O_Y2 + (unsigned)((tb * 15 + 14) * 2 + hl) * KB + 256 + e * 16) = z; }
    }
    if (w == 0) TS(b, 4004);
    u4 Bop[4][8];
    unsigned char* AL = lds + 32768 + w * 16384;
    if (mm) {
#pragma unroll
        for (int i = 0; i < 16; ++i) { const int r = 2 * i + (lane >> 5); *(u4*)(AL + r * 512 + (((lane & 31) ^ pswz(r)) << 4)) = Ast[i]; }
        {
            const int k2 = w + 4;
#pragma unroll
            for (int i = 0; i < 16; ++i) Ast[i] = ldx16(W1 + ((size_t)(32 * k2 + 2 * i + (lane >> 5)) * NH + h) * 256 + (lane & 31) * 8);
        }
        if (isA) {
#pragma unroll
            for (int i = 0; i < 8; ++i) { const int r = 16 * w + 2 * i + (lane >> 5); *(u4*)(lds + r * 512 + (((lane & 31) ^ pswz(r)) << 4)) = Bsrc[i]; }
        } else {
#pragma unroll
            for (int i = 0; i < 8; ++i) *(u4*)(lds + 110592 + (64 * w + 8 * i + (lane >> 3)) * 144 + (lane & 7) * 16) = Bsrc[i];
        }
    }
    bar();
    if (w == 0) TS(b, 4005);
    if (mm) {
        if (!isA) {
            const us* R = (const us*)(lds + 110592);
#pragma unroll
            for (int q = 0; q < 8; ++q) {
                unsigned t[8];
#pragma unroll
                for (int j = 0; j < 8; ++j) t[j] = R[(32 * q + 8 * g + j) * 72 + 16 * w + n];
                *(u4*)(lds + ((w * 8 + q) * 64 + lane) * 16) = u4{t[0] | (t[1] << 16), t[2] | (t[3] << 16), t[4] | (t[5] << 16), t[6] | (t[7] << 16)};
            }
        }
    }
    bar();
    if (mm) {
#pragma unroll
        for (int c = 0; c < 4; ++c)
#pragma unroll
            for (int q = 0; q < 8; ++q) {
                const int r = 32 * (c >> 1) + 8 * (n >> 2) + 4 * (c & 1) + (n & 3);
                Bop[c][q] = isA ? *(const u4*)(lds + r * 512 + (((4 * q + g) ^ pswz(r)) << 4)) : *(const u4*)(lds + ((c * 8 + q) * 64 + lane) * 16);
            }
    }
    if (w == 0) TS(b, 4006);
    if (mm) {
        const float sc = isA ? 0.0625f * 1.4426950408889634f : 1.0f;
        u4 Aop[2][8];
        const int ra0 = 8 * (n >> 2) + (n & 3), ra1 = ra0 + 4;
        __builtin_amdgcn_wave_barrier();
#pragma unroll
        for (int q = 0; q < 8; ++q) Aop[0][q] = *(const u4*)(AL + ra0 * 512 + (((4 * q + g) ^ pswz(ra0)) << 4));
#pragma unroll 1
        for (int k = w; k < 14; k += 4) {
            f4 acc[4][2];
#pragma unroll
            for (int c = 0; c < 4; ++c) { acc[c][0] = f4{0, 0, 0, 0}; acc[c][1] = f4{0, 0, 0, 0}; }
            __builtin_amdgcn_sched_barrier(0);
            PSTAMP(0);
            const int k2 = k + 8 < 14 ? k + 8 : k;
            const us* Wn = W1 + ((size_t)(32 * k2 + (lane >> 5)) * NH + h) * 256 + (lane & 31) * 8;
#pragma unroll
            for (int q = 0; q < 8; ++q) Aop[1][q] = *(const u4*)(AL + ra1 * 512 + (((4 * q + g) ^ pswz(ra1)) << 4));
#pragma unroll
            for (int j = 0; j < 8; ++j) {
                const int i = (j >> 1) * 4 + (j & 1), r = 2 * i + (lane >> 5);
                *(u4*)(AL + r * 512 + (((lane & 31) ^ pswz(r)) << 4)) = Ast[i];
                Ast[i] = ldx16(Wn + (size_t)(2 * i) * NH * 256);
            }
#pragma unroll
            for (int q = 0; q < 8; ++q)
#pragma unroll
                for (int c = 0; c < 4; ++c) acc[c][0] = mfma32(Aop[0][q], Bop[c][q], acc[c][0]);
#pragma unroll
            for (int i = 0; i < 8; ++i) { __builtin_amdgcn_sched_group_barrier(0x100, 1, 0); __builtin_amdgcn_sched_group_barrier(0x008, 1, 0); }
#pragma unroll
            for (int i = 0; i < 8; ++i) { __builtin_amdgcn_sched_group_barrier(0x200, 1, 0); __builtin_amdgcn_sched_group_barrier(0x020, 1, 0); __builtin_amdgcn_sched_group_barrier(0x008, 3, 0); }
            __builtin_amdgcn_sched_barrier(0);
            __builtin_amdgcn_wave_barrier();
            PSTAMP(1);
            u4 An[8];
#pragma unroll
            for (int j = 0; j < 8; ++j) {
                const int i = (j >> 1) * 4 + 2 + (j & 1), r = 2 * i + (lane >> 5);
                *(u4*)(AL + r * 512 + (((lane & 31) ^ pswz(r)) << 4)) = Ast[i];
                Ast[i] = ldx16(Wn + (size_t)(2 * i) * NH * 256);
                An[j] = *(const u4*)(AL + ra0 * 512 + (((4 * j + g) ^ pswz(ra0)) << 4));
            }
#pragma unroll
            for (int q = 0; q < 8; ++q)
#pragma unroll
                for (int c = 0; c < 4; ++c) acc[c][1] = mfma32(Aop[1][q], Bop[c][q], acc[c][1]);
#pragma unroll
            for (int i = 0; i < 8; ++i) { __builtin_amdgcn_sched_group_barrier(0x200, 1, 0); __builtin_amdgcn_sched_group_barrier(0x020, 1, 0); __builtin_amdgcn_sched_group_barrier(0x100, 1, 0); __builtin_amdgcn_sched_group_barrier(0x008, 4, 0); }
            __builtin_amdgcn_sched_barrier(0);
            PSTAMP(2);
#pragma unroll
            for (int q = 0; q < 8; ++q) Aop[0][q] = An[q];
#pragma unroll
            for (int c = 0; c < 4; ++c) {
                u4 v = {pk2(acc[c][0][0] * sc, acc[c][0][1] * sc), pk2(acc[c][0][2] * sc, acc[c][0][3] * sc),
                        pk2(acc[c][1][0] * sc, acc[c][1][1] * sc), pk2(acc[c][1][2] * sc, acc[c][1][3] * sc)};
                unsigned off;
                if (isA) { const int u = 2 * up + (c >> 1), tt = c & 1; off = O_AF + (unsigned)(((h * 14 + u) * 2 + tt) * 14 + k) * KB; }
                else { const int ft = 4 * up + c; off = O_BF + (unsigned)((h * 28 + ft) * 14 + k) * KB; }
                PREP_ST(off + lane * 16, v);
            }
            PSTAMP(3);
        }
    } else if (b == 248 && w < 2) {
        const int pg = w;
        unsigned best[2] = {~0u, ~0u};
        for (int rep = 0; rep < 4; ++rep)
            for (int par = 0; par < 2; ++par) st4c(rs, O_EX + (unsigned)(pg * 32 * 4 + 3) * 16384u + par * 8192u + 7168u + rep * 256u, 0u);
        vm0();
        for (int rep = 0; rep < 4; ++rep)
            for (int par = 0; par < 2; ++par) {
                const unsigned off = O_EX + (unsigned)(pg * 32 * 4 + 3) * 16384u + par * 8192u + 7168u + rep * 256u;
                unsigned long long t0, t1; unsigned v;
                const unsigned* gp = (const unsigned*)(P.ws + off);
                asm volatile("s_memtime %0\n\ts_waitcnt lgkmcnt(0)\n\tglobal_load_dword %2, %3, off sc1\n\ts_waitcnt vmcnt(0)\n\ts_memtime %1\n\ts_waitcnt lgkmcnt(0)"
                             : "=&s"(t0), "=&s"(t1), "=&v"(v) : "v"(gp) : "memory");
                unsigned dt = (unsigned)(t1 - t0);
                best[par] = dt < best[par] ? dt : best[par];
            }
        if (lane == 0) { *(unsigned*)(P.ws + O_PAR + pg * 4) = best[1] < best[0] ? 1u : 0u;
                         *(unsigned*)(P.ws + O_PAR + 16 + pg * 8) = best[0]; *(unsigned*)(P.ws + O_PAR + 20 + pg * 8) = best[1]; }
    }
    asm volatile("" :: "v"(tch), "v"(tch2));
    if (w == 0) TS(b, 4003);
}

DEV u4 mk_rsrc(const void* p) { const unsigned long long a = (unsigned long long)p; return u4{(unsigned)a, (unsigned)(a >> 32) & 0xFFFFu, 0x10000000u, 0x00020000u}; }
#define TS_OPS
constexpr unsigned long long M16 = 0x00FF00FFull, M8 = 0x00FF00FF00FF00FFull;

DEV void role_A(const Params& P, unsigned char* lds, int ci, unsigned par2, int w, int lane) {
    const int n = lane & 15, g = lane >> 4;
    const int h = ci / 5, cj = ci % 5, u0 = 3 * cj, nu = cj < 4 ? 3 : 2;
    const int k0 = w < 2 ? 4 * w : 8 + 3 * (w - 2), nk = w < 2 ? 4 : 3;
    if (threadIdx.x < 64) { ((u4*)(lds + 7168))[lane] = u4{0, 0, 0, 0}; ((u4*)(lds + 8192 + 7168))[lane] = u4{0, 0, 0, 0}; }
    __syncthreads();
    const int ka = k0 + (n >> 3), kb = (k0 + 2 + (n >> 3) < k0 + nk) ? k0 + 2 + (n >> 3) : k0 + 2;
    const unsigned voa = (unsigned)((4 * ka + g) * 8 + (n & 7)) * 16u, vob = (unsigned)((4 * kb + g) * 8 + (n & 7)) * 16u;
    const unsigned vcf = (unsigned)((4 * u0 + g) * 8 + (n & 7)) * 16u, vcf2 = nu == 3 ? vcf + 1024u : vcf;
    const unsigned vx0 = (unsigned)(n * DM + 32 * u0 + 8 * g) * 2u, vx1 = vx0 + 16u * DM * 2u;
    const unsigned vx20 = nu == 3 ? vx0 + 128u : vx0, vx21 = nu == 3 ? vx1 + 128u : vx1;
    const int e = h * 5 + cj;
    unsigned vwrm;
    {
        const int x = ci & 3, q = (ci >> 2) * 4 + w;
        int id = q * 23 + (lane < 23 ? lane : 0);
        id = id < 1792 ? id : 1791;
        if (id < 896) { const int jj = id >> 7; vwrm = O_VF + (unsigned)((x + 4 * jj) * 16) * KB + (unsigned)(id & 127) * 128u; }
        else { const int i2 = id - 896, hh = i2 / 224, ln = i2 % 224; vwrm = O_GF + (unsigned)((x + 4 * hh) * 28) * KB + (unsigned)ln * 128u; }
    }
    const u4 rs = mk_rsrc(P.ws);
    asm volatile(
    "s_mov_b32 s60, 0\n"
    "s_mov_b32 s62, %[spin]\n"
    "v_mov_b32 v120, %[voa]\n"
    "v_mov_b32 v121, %[vob]\n"
    "v_mov_b32 v122, %[vcf]\n"
    "v_mov_b32 v123, %[vcf2]\n"
    "s_mov_b64 s[68:69], %[xb]\n"
    "s_mov_b32 s70, %[ssxm]\n"
    "s_mov_b32 s71, %[sfl]\n"
    "s_mov_b32 s72, 917504\n"
    "v_accvgpr_write_b32 a0, 0\n"
    "v_accvgpr_write_b32 a1, 0\n"
    "v_accvgpr_write_b32 a2, 0\n"
    "v_accvgpr_write_b32 a3, 0\n"
    "v_accvgpr_write_b32 a4, 0\n"
    "v_accvgpr_write_b32 a5, 0\n"
    "v_accvgpr_write_b32 a6, 0\n"
    "v_accvgpr_write_b32 a7, 0\n"
    "v_accvgpr_write_b32 a8, 0\n"
    "v_accvgpr_write_b32 a9, 0\n"
    "v_accvgpr_write_b32 a10, 0\n"
    "v_accvgpr_write_b32 a11, 0\n"
    "v_accvgpr_write_b32 a12, 0\n"
    "v_accvgpr_write_b32 a13, 0\n"
    "v_accvgpr_write_b32 a14, 0\n"
    "v_accvgpr_write_b32 a15, 0\n"
    "v_accvgpr_write_b32 a16, 0\n"
    "v_accvgpr_write_b32 a17, 0\n"
    "v_accvgpr_write_b32 a18, 0\n"
    "v_accvgpr_write_b32 a19, 0\n"
    "v_accvgpr_write_b32 a20, 0\n"
    "v_accvgpr_write_b32 a21, 0\n"
    "v_accvgpr_write_b32 a22, 0\n"
    "v_accvgpr_write_b32 a23, 0\n"
    "v_accvgpr_write_b32 a24, 0\n"
    "v_accvgpr_write_b32 a25, 0\n"
    "v_accvgpr_write_b32 a26, 0\n"
    "v_accvgpr_write_b32 a27, 0\n"
    "v_accvgpr_write_b32 a28, 0\n"
    "v_accvgpr_write_b32 a29, 0\n"
    "v_accvgpr_write_b32 a30, 0\n"
    "v_accvgpr_write_b32 a31, 0\n"
    "v_accvgpr_write_b32 a32, 0\n"
    "v_accvgpr_write_b32 a33, 0\n"
    "v_accvgpr_write_b32 a34, 0\n"
    "v_accvgpr_write_b32 a35, 0\n"
    "v_accvgpr_write_b32 a36, 0\n"
    "v_accvgpr_write_b32 a37, 0\n"
    "v_accvgpr_write_b32 a38, 0\n"
    "v_accvgpr_write_b32 a39, 0\n"
    "v_accvgpr_write_b32 a40, 0\n"
    "v_accvgpr_write_b32 a41, 0\n"
    "v_accvgpr_write_b32 a42, 0\n"
    "v_accvgpr_write_b32 a43, 0\n"
    "v_accvgpr_write_b32 a44, 0\n"
    "v_accvgpr_write_b32 a45, 0\n"
    "v_accvgpr_write_b32 a46, 0\n"
    "v_accvgpr_write_b32 a47, 0\n"
    "v_accvgpr_write_b32 a48, 0\n"
    "v_accvgpr_write_b32 a49, 0\n"
    "v_accvgpr_write_b32 a50, 0\n"
    "v_accvgpr_write_b32 a51, 0\n"
    "v_accvgpr_write_b32 a52, 0\n"
    "v_accvgpr_write_b32 a53, 0\n"
    "v_accvgpr_write_b32 a54, 0\n"
    "v_accvgpr_write_b32 a55, 0\n"
    "v_accvgpr_write_b32 a56, 0\n"
    "v_accvgpr_write_b32 a57, 0\n"
    "v_accvgpr_write_b32 a58, 0\n"
    "v_accvgpr_write_b32 a59, 0\n"
    "v_accvgpr_write_b32 a60, 0\n"
    "v_accvgpr_write_b32 a61, 0\n"
    "v_accvgpr_write_b32 a62, 0\n"
    "v_accvgpr_write_b32 a63, 0\n"
    "v_accvgpr_write_b32 a64, 0\n"
    "v_accvgpr_write_b32 a65, 0\n"
    "v_accvgpr_write_b32 a66, 0\n"
    "v_accvgpr_write_b32 a67, 0\n"
    "v_accvgpr_write_b32 a68, 0\n"
    "v_accvgpr_write_b32 a69, 0\n"
    "v_accvgpr_write_b32 a70, 0\n"
    "v_accvgpr_write_b32 a71, 0\n"
    "v_accvgpr_write_b32 a72, 0\n"
    "v_accvgpr_write_b32 a73, 0\n"
    "v_accvgpr_write_b32 a74, 0\n"
    "v_accvgpr_write_b32 a75, 0\n"
    "v_accvgpr_write_b32 a76, 0\n"
    "v_accvgpr_write_b32 a77, 0\n"
    "v_accvgpr_write_b32 a78, 0\n"
    "v_accvgpr_write_b32 a79, 0\n"
    "v_accvgpr_write_b32 a80, 0\n"
    "v_accvgpr_write_b32 a81, 0\n"
    "v_accvgpr_write_b32 a82, 0\n"
    "v_accvgpr_write_b32 a83, 0\n"
    "v_accvgpr_write_b32 a84, 0\n"
    "v_accvgpr_write_b32 a85, 0\n"
    "v_accvgpr_write_b32 a86, 0\n"
    "v_accvgpr_write_b32 a87, 0\n"
    "v_accvgpr_write_b32 a88, 0\n"
    "v_accvgpr_write_b32 a89, 0\n"
    "v_accvgpr_write_b32 a90, 0\n"
    "v_accvgpr_write_b32 a91, 0\n"
    "v_accvgpr_write_b32 a92, 0\n"
    "v_accvgpr_write_b32 a93, 0\n"
    "v_accvgpr_write_b32 a94, 0\n"
    "v_accvgpr_write_b32 a95, 0\n"
    "v_mov_b32 v128, -1\n"
    "v_mov_b32 v129, -1\n"
    "v_mov_b32 v130, -1\n"
    "v_mov_b32 v131, -1\n"
    "s_mov_b32 s64, %[sw]\n"
    "buffer_load_dwordx4 a[0:3], %[vl16], %[rs], s64 offen offset:0\n"
    "buffer_load_dwordx4 a[4:7], %[vl16], %[rs], s64 offen offset:1024\n"
    "buffer_load_dwordx4 a[8:11], %[vl16], %[rs], s64 offen offset:2048\n"
    "s_cmp_lt_u32 %[w], 2\n"
    "s_cbranch_scc0 LAw00_%=\n"
    "buffer_load_dwordx4 a[12:15], %[vl16], %[rs], s64 offen offset:3072\n"
    "LAw00_%=:\n"
    "s_add_u32 s64, s64, 14336\n"
    "buffer_load_dwordx4 a[16:19], %[vl16], %[rs], s64 offen offset:0\n"
    "buffer_load_dwordx4 a[20:23], %[vl16], %[rs], s64 offen offset:1024\n"
    "buffer_load_dwordx4 a[24:27], %[vl16], %[rs], s64 offen offset:2048\n"
    "s_cmp_lt_u32 %[w], 2\n"
    "s_cbranch_scc0 LAw01_%=\n"
    "buffer_load_dwordx4 a[28:31], %[vl16], %[rs], s64 offen offset:3072\n"
    "LAw01_%=:\n"
    "s_add_u32 s64, s64, 14336\n"
    "buffer_load_dwordx4 a[32:35], %[vl16], %[rs], s64 offen offset:0\n"
    "buffer_load_dwordx4 a[36:39], %[vl16], %[rs], s64 offen offset:1024\n"
    "buffer_load_dwordx4 a[40:43], %[vl16], %[rs], s64 offen offset:2048\n"
    "s_cmp_lt_u32 %[w], 2\n"
    "s_cbranch_scc0 LAw10_%=\n"
    "buffer_load_dwordx4 a[44:47], %[vl16], %[rs], s64 offen offset:3072\n"
    "LAw10_%=:\n"
    "s_add_u32 s64, s64, 14336\n"
    "buffer_load_dwordx4 a[48:51], %[vl16], %[rs], s64 offen offset:0\n"
    "buffer_load_dwordx4 a[52:55], %[vl16], %[rs], s64 offen offset:1024\n"
    "buffer_load_dwordx4 a[56:59], %[vl16], %[rs], s64 offen offset:2048\n"
    "s_cmp_lt_u32 %[w], 2\n"
    "s_cbranch_scc0 LAw11_%=\n"
    "buffer_load_dwordx4 a[60:63], %[vl16], %[rs], s64 offen offset:3072\n"
    "LAw11_%=:\n"
    "s_add_u32 s64, s64, 14336\n"
    "s_cmp_lt_u32 %[nu], 3\n"
    "s_cbranch_scc1 LAwskip_%=\n"
    "buffer_load_dwordx4 a[64:67], %[vl16], %[rs], s64 offen offset:0\n"
    "buffer_load_dwordx4 a[68:71], %[vl16], %[rs], s64 offen offset:1024\n"
    "buffer_load_dwordx4 a[72:75], %[vl16], %[rs], s64 offen offset:2048\n"
    "s_cmp_lt_u32 %[w], 2\n"
    "s_cbranch_scc0 LAw20_%=\n"
    "buffer_load_dwordx4 a[76:79], %[vl16], %[rs], s64 offen offset:3072\n"
    "LAw20_%=:\n"
    "s_add_u32 s64, s64, 14336\n"
    "buffer_load_dwordx4 a[80:83], %[vl16], %[rs], s64 offen offset:0\n"
    "buffer_load_dwordx4 a[84:87], %[vl16], %[rs], s64 offen offset:1024\n"
    "buffer_load_dwordx4 a[88:91], %[vl16], %[rs], s64 offen offset:2048\n"
    "s_cmp_lt_u32 %[w], 2\n"
    "s_cbranch_scc0 LAw21_%=\n"
    "buffer_load_dwordx4 a[92:95], %[vl16], %[rs], s64 offen offset:3072\n"
    "LAw21_%=:\n"
    "s_add_u32 s64, s64, 14336\n"
    "LAwskip_%=:\n"
    "global_load_dwordx4 v[68:71], %[vx0], s[68:69] offset:0\n"
    "global_load_dwordx4 v[72:75], %[vx1], s[68:69] offset:0\n"
    "global_load_dwordx4 v[76:79], %[vx0], s[68:69] offset:64\n"
    "global_load_dwordx4 v[80:83], %[vx1], s[68:69] offset:64\n"
    "global_load_dwordx4 v[84:87], %[vx20], s[68:69]\n"
    "global_load_dwordx4 v[88:91], %[vx21], s[68:69]\n"
    "s_lshr_b32 s64, s60, 5\n"
    "s_lshr_b32 s64, %[par2], s64\n"
    "s_and_b32 s64, s64, 1\n"
    "s_lshl_b32 s64, s64, 13\n"
    "s_lshl_b32 s61, s60, 16\n"
    "s_add_u32 s61, s61, s64\n"
    "s_add_u32 s61, s61, %[oex]\n"
    "s_memrealtime s[78:79]\n"
    "s_waitcnt lgkmcnt(0)\n"
    "s_mov_b32 s76, s78\n"
    "s_mov_b32 s98, 0\n"
    "s_mov_b32 s99, %[pa0]\n"
    "LAtop_%=:\n"
    "s_mov_b32 s63, 0\n"
    "s_add_u32 s65, s76, s98\n"
    "s_mov_b32 s80, 2048\n"
    "LAtw_%=:\n"
    "s_memrealtime s[78:79]\n"
    "s_sub_u32 s80, s80, 1\n"
    "s_cbranch_scc1 LAtx_%=\n"
    "s_waitcnt lgkmcnt(0)\n"
    "s_sub_u32 s64, s65, s78\n"
    "s_cmp_gt_i32 s64, 3\n"
    "s_cbranch_scc1 LAtw_%=\n"
    "LAtv_%=:\n"
    "s_cmp_gt_i32 s64, 0\n"
    "s_cbranch_scc0 LAtx_%=\n"
    "s_sub_u32 s64, s64, 1\n"
    "s_nop 1\n"
    "s_branch LAtv_%=\n"
    "LAtx_%=:\n"
    "s_branch LApmf_%=\n"
    "LApmr_%=:\n"
    "s_mov_b32 s63, 1\n"
    "s_sub_u32 s62, s62, 1\n"
    "s_cbranch_scc1 LApmg_%=\n"
    "LApmf_%=:\n"
    "buffer_load_dwordx4 v[0:3], %[voa], %[rs], s61 offen sc1\n"
    "buffer_load_dwordx4 v[4:7], %[vob], %[rs], s61 offen sc1\n"
    "buffer_load_dword v124, %[vstp], %[rs], s61 offen sc1\n"
    "s_waitcnt vmcnt(0)\n"
    "v_max3_u32 v100, v0, v1, v2\n"
    "v_max3_u32 v101, v3, v4, v5\n"
    "v_max3_u32 v100, v100, v6, v7\n"
    "v_max3_u32 v100, v100, v101, v124\n"
    "v_cmp_eq_u32 vcc, -1, v100\n"
    "s_cbranch_vccnz LApmr_%=\n"
    "LApmd_%=:\n"
    "s_cmp_eq_u32 s60, 63\n"
    "s_cbranch_scc0 LAe63_%=\n"
    "s_cmp_eq_u32 %[w], 1\n"
    "s_cbranch_scc0 LAe63_%=\n"
    "s_mov_b64 exec, 1\n"
    "v_mov_b32 v116, s60\n"
    "v_mov_b32 v117, 0\n"
    "buffer_store_dword v116, v117, %[rs], s71 offen sc1\n"
    "s_mov_b64 exec, -1\n"
    "LAe63_%=:\n"
    "ds_write_b128 v120, v[0:3]\n"
    "ds_write_b128 v121, v[4:7]\n"
    "buffer_load_dword v125, %[vwrm], %[rs], s72 offen sc1\n"
    "v_mfma_f32_16x16x32_bf16 v[16:19], a[0:3], v[0:3], 0\n"
    "v_mov_b32_dpp v8, v0 row_ror:8 row_mask:0xf bank_mask:0xf\n"
    "v_mov_b32_dpp v9, v1 row_ror:8 row_mask:0xf bank_mask:0xf\n"
    "v_mfma_f32_16x16x32_bf16 v[20:23], a[16:19], v[0:3], 0\n"
    "v_mov_b32_dpp v10, v2 row_ror:8 row_mask:0xf bank_mask:0xf\n"
    "v_mov_b32_dpp v11, v3 row_ror:8 row_mask:0xf bank_mask:0xf\n"
    "v_mfma_f32_16x16x32_bf16 v[16:19], a[8:11], v[4:7], v[16:19]\n"
    "v_mov_b32_dpp v12, v4 row_ror:8 row_mask:0xf bank_mask:0xf\n"
    "v_mov_b32_dpp v13, v5 row_ror:8 row_mask:0xf bank_mask:0xf\n"
    "v_mfma_f32_16x16x32_bf16 v[20:23], a[24:27], v[4:7], v[20:23]\n"
    "v_mov_b32_dpp v14, v6 row_ror:8 row_mask:0xf bank_mask:0xf\n"
    "v_mov_b32_dpp v15, v7 row_ror:8 row_mask:0xf bank_mask:0xf\n"
    "s_waitcnt lgkmcnt(0)\n"
    "s_barrier\n"
    "ds_read_b128 v[52:55], v122\n"
    "ds_read_b128 v[56:59], v122 offset:512\n"
    "ds_read_b128 v[60:63], v123\n"
    "v_mfma_f32_16x16x32_bf16 v[16:19], a[4:7], v[8:11], v[16:19]\n"
    "v_mfma_f32_16x16x32_bf16 v[20:23], a[20:23], v[8:11], v[20:23]\n"
    "v_mfma_f32_16x16x32_bf16 v[16:19], a[12:15], v[12:15], v[16:19]\n"
    "v_mfma_f32_16x16x32_bf16 v[20:23], a[28:31], v[12:15], v[20:23]\n"
    "v_mfma_f32_16x16x32_bf16 v[24:27], a[32:35], v[0:3], 0\n"
    "v_readlane_b32 s66, v124, 0\n"
    "v_readlane_b32 s67, v124, 16\n"
    "v_mfma_f32_16x16x32_bf16 v[28:31], a[48:51], v[0:3], 0\n"
    "v_readlane_b32 s80, v124, 32\n"
    "v_readlane_b32 s78, v124, 48\n"
    "v_mfma_f32_16x16x32_bf16 v[24:27], a[40:43], v[4:7], v[24:27]\n"
    "s_sub_u32 s66, s66, s76\n"
    "s_sub_u32 s67, s67, s76\n"
    "s_sub_u32 s80, s80, s76\n"
    "s_sub_u32 s78, s78, s76\n"
    "v_mfma_f32_16x16x32_bf16 v[28:31], a[56:59], v[4:7], v[28:31]\n"
    "s_max_i32 s66, s66, s67\n"
    "s_max_i32 s80, s80, s78\n"
    "s_max_i32 s66, s66, s80\n"
    "v_mfma_f32_16x16x32_bf16 v[24:27], a[36:39], v[8:11], v[24:27]\n"
    "s_add_i32 s66, s66, %[c0]\n"
    "s_add_i32 s67, s66, %[rr]\n"
    "s_add_i32 s80, s66, %[r1]\n"
    "s_cmp_gt_i32 s66, 0\n"
    "s_cselect_b32 s78, %[pinc], 0\n"
    "s_cmp_gt_i32 s66, %[d1]\n"
    "s_cselect_b32 s67, s67, s80\n"
    "s_cselect_b32 s78, %[pinc2], s78\n"
    "s_cmp_ge_u32 s60, %[dst]\n"
    "s_cselect_b32 s80, -1, 0\n"
    "s_cmp_lt_i32 s66, %[nd2]\n"
    "s_cselect_b32 s78, s80, s78\n"
    "s_cmp_le_u32 s60, %[fst]\n"
    "s_cselect_b32 s78, 0, s78\n"
    "s_add_i32 s99, s99, s78\n"
    "s_max_i32 s99, s99, 0\n"
    "s_min_i32 s99, s99, %[pcap]\n"
    "s_cmp_gt_i32 s66, 0\n"
    "s_cselect_b32 s66, s67, 0\n"
    "s_cmp_eq_u32 s60, 1\n"
    "s_cselect_b32 s66, s67, s66\n"
    "s_cmp_eq_u32 s60, 0\n"
    "s_cselect_b32 s66, 0, s66\n"
    "s_cmp_eq_u32 s66, 0\n"
    "s_cselect_b32 s80, s63, 0\n"
    "s_lshl_b32 s80, s80, 2\n"
    "s_add_u32 s98, s98, s80\n"
    "s_min_u32 s98, s98, %[mcap]\n"
    "s_add_u32 s77, s76, s66\n"
    "s_add_u32 s76, s77, %[pp]\n"
    "s_cmp_ge_u32 s60, %[psw]\n"
    "s_cselect_b32 s67, %[pdp], 0\n"
    "s_sub_u32 s76, s76, s67\n"
    "s_add_u32 s76, s76, s99\n"
    "v_mfma_f32_16x16x32_bf16 v[28:31], a[52:55], v[8:11], v[28:31]\n"
    "v_mfma_f32_16x16x32_bf16 v[24:27], a[44:47], v[12:15], v[24:27]\n"
    "v_mfma_f32_16x16x32_bf16 v[28:31], a[60:63], v[12:15], v[28:31]\n"
    "v_mfma_f32_16x16x32_bf16 v[32:35], a[64:67], v[0:3], 0\n"
    "v_cvt_pk_bf16_f32 v40, v16, v17\n"
    "v_cvt_pk_bf16_f32 v41, v18, v19\n"
    "v_mfma_f32_16x16x32_bf16 v[32:35], a[72:75], v[4:7], v[32:35]\n"
    "v_cvt_pk_bf16_f32 v42, v20, v21\n"
    "v_cvt_pk_bf16_f32 v43, v22, v23\n"
    "v_mfma_f32_16x16x32_bf16 v[32:35], a[68:71], v[8:11], v[32:35]\n"
    "s_waitcnt lgkmcnt(0)\n"
    "v_mfma_f32_16x16x32_bf16 v[32:35], a[76:79], v[12:15], v[32:35]\n"
    "v_mfma_f32_16x16x32_bf16 v[64:67], v[40:43], v[52:55], 0\n"
    "v_mfma_f32_16x16x32_bf16 v[36:39], a[80:83], v[0:3], 0\n"
    "v_cvt_pk_bf16_f32 v44, v24, v25\n"
    "v_cvt_pk_bf16_f32 v45, v26, v27\n"
    "v_mfma_f32_16x16x32_bf16 v[36:39], a[88:91], v[4:7], v[36:39]\n"
    "v_cvt_pk_bf16_f32 v46, v28, v29\n"
    "v_cvt_pk_bf16_f32 v47, v30, v31\n"
    "v_mfma_f32_16x16x32_bf16 v[36:39], a[84:87], v[8:11], v[36:39]\n"
    "v_mfma_f32_16x16x32_bf16 v[36:39], a[92:95], v[12:15], v[36:39]\n"
    "v_mfma_f32_16x16x32_bf16 v[64:67], v[44:47], v[56:59], v[64:67]\n"
    "v_cvt_pk_bf16_f32 v48, v32, v33\n"
    "v_cvt_pk_bf16_f32 v49, v34, v35\n"
    "s_nop 4\n"
    "v_cvt_pk_bf16_f32 v50, v36, v37\n"
    "v_cvt_pk_bf16_f32 v51, v38, v39\n"
    "s_nop 1\n"
    "v_mfma_f32_16x16x32_bf16 v[64:67], v[48:51], v[60:63], v[64:67]\n"
    "s_mov_b64 exec, %[m16]\n"
    "s_nop 6\n"
    "ds_write_b128 %[vredw], v[64:67]\n"
    "s_mov_b64 exec, -1\n"
    "s_waitcnt lgkmcnt(0)\n"
    "s_barrier\n"
    "s_cmp_eq_u32 %[w], 0\n"
    "s_cbranch_scc0 LAnopub_%=\n"
    "s_mov_b64 exec, 0xffff\n"
    "ds_read_b128 v[100:103], %[vredr]\n"
    "ds_read_b128 v[104:107], %[vredr] offset:256\n"
    "ds_read_b128 v[108:111], %[vredr] offset:512\n"
    "ds_read_b128 v[112:115], %[vredr] offset:768\n"
    "s_add_u32 s65, s61, %[ssm]\n"
    "s_waitcnt lgkmcnt(2)\n"
    "v_pk_add_f32 v[100:101], v[100:101], v[104:105]\n"
    "v_pk_add_f32 v[102:103], v[102:103], v[106:107]\n"
    "s_waitcnt lgkmcnt(0)\n"
    "v_pk_add_f32 v[108:109], v[108:109], v[112:113]\n"
    "v_pk_add_f32 v[110:111], v[110:111], v[114:115]\n"
    "v_pk_add_f32 v[100:101], v[100:101], v[108:109]\n"
    "v_pk_add_f32 v[102:103], v[102:103], v[110:111]\n"
    "v_cvt_pk_bf16_f32 v100, v100, v101\n"
    "v_cvt_pk_bf16_f32 v101, v102, v103\n"
    "buffer_store_dwordx2 v[100:101], %[vsmo], %[rs], s65 offen sc1\n"
    "s_mov_b64 exec, -1\n"
    "LAnopub_%=:\n"
    "v_mfma_f32_16x16x32_bf16 v[92:95], v[40:43], v[68:71], 0\n"
    "v_mfma_f32_16x16x32_bf16 v[96:99], v[40:43], v[72:75], 0\n"
    "v_mfma_f32_16x16x32_bf16 v[92:95], v[44:47], v[76:79], v[92:95]\n"
    "v_mfma_f32_16x16x32_bf16 v[96:99], v[44:47], v[80:83], v[96:99]\n"
    "v_mfma_f32_16x16x32_bf16 v[92:95], v[48:51], v[84:87], v[92:95]\n"
    "v_mfma_f32_16x16x32_bf16 v[96:99], v[48:51], v[88:91], v[96:99]\n"
    "v_xor_b32 v120, 0x2000, v120\n"
    "v_xor_b32 v121, 0x2000, v121\n"
    "v_xor_b32 v122, 0x2000, v122\n"
    "v_xor_b32 v123, 0x2000, v123\n"
    "s_add_u32 s68, s68, 28672\n"
    "s_addc_u32 s69, s69, 0\n"
    "s_mov_b32 exec_hi, 0\n"
    "s_nop 1\n"
    "ds_write_b128 %[vred2w], v[92:95]\n"
    "ds_write_b128 %[vred2w], v[96:99] offset:512\n"
    "s_mov_b64 exec, -1\n"
    "s_cmp_lt_u32 s60, 63\n"
    "s_cbranch_scc0 LAnox_%=\n"
    "global_load_dwordx4 v[68:71], %[vx0], s[68:69] offset:0\n"
    "global_load_dwordx4 v[72:75], %[vx1], s[68:69] offset:0\n"
    "global_load_dwordx4 v[76:79], %[vx0], s[68:69] offset:64\n"
    "global_load_dwordx4 v[80:83], %[vx1], s[68:69] offset:64\n"
    "global_load_dwordx4 v[84:87], %[vx20], s[68:69]\n"
    "global_load_dwordx4 v[88:91], %[vx21], s[68:69]\n"
    "LAnox_%=:\n"
    "s_waitcnt lgkmcnt(0)\n"
    "s_barrier\n"
    "s_cmp_eq_u32 %[w], 1\n"
    "s_cbranch_scc0 LAnosx_%=\n"
    "ds_read_b128 v[100:103], %[vred2r]\n"
    "ds_read_b128 v[104:107], %[vred2r] offset:1024\n"
    "ds_read_b128 v[108:111], %[vred2r] offset:2048\n"
    "ds_read_b128 v[112:115], %[vred2r] offset:3072\n"
    "s_cmp_eq_u32 s60, 0\n"
    "s_cbranch_scc1 LAnofl_%=\n"
    "s_mov_b64 exec, 1\n"
    "v_mov_b32 v116, s60\n"
    "v_mov_b32 v117, 0\n"
    "buffer_store_dword v116, v117, %[rs], s71 offen sc1\n"
    "s_mov_b64 exec, -1\n"
    "LAnofl_%=:\n"
    "s_waitcnt lgkmcnt(0)\n"
    "v_pk_add_f32 v[100:101], v[100:101], v[104:105]\n"
    "v_pk_add_f32 v[102:103], v[102:103], v[106:107]\n"
    "v_pk_add_f32 v[108:109], v[108:109], v[112:113]\n"
    "v_pk_add_f32 v[110:111], v[110:111], v[114:115]\n"
    "v_pk_add_f32 v[100:101], v[100:101], v[108:109]\n"
    "v_pk_add_f32 v[102:103], v[102:103], v[110:111]\n"
    "buffer_store_dwordx4 v[100:103], %[vsxo], %[rs], s70 offen sc1\n"
    "LAnosx_%=:\n"
    "s_add_u32 s60, s60, 1\n"
    "s_add_u32 s70, s70, 81920\n"
    "s_add_u32 s72, s72, 458752\n"
    "s_min_u32 s72, s72, 28442624\n"
    "s_cmp_eq_u32 s60, 64\n"
    "s_cbranch_scc1 LAend_%=\n"
    "s_lshr_b32 s64, s60, 5\n"
    "s_lshr_b32 s64, %[par2], s64\n"
    "s_and_b32 s64, s64, 1\n"
    "s_lshl_b32 s64, s64, 13\n"
    "s_lshl_b32 s61, s60, 16\n"
    "s_add_u32 s61, s61, s64\n"
    "s_add_u32 s61, s61, %[oex]\n"
    "s_branch LAtop_%=\n"
    "LApmg_%=:\n"
    "s_mov_b32 s62, 0\n"
    "s_branch LApmd_%=\n"
    "LAend_%=:\n"
    "s_waitcnt vmcnt(0)\n"
    "s_cmp_eq_u32 %[w], 1\n"
    "s_cbranch_scc0 LAfin_%=\n"
    "s_mov_b64 exec, 1\n"
    "v_mov_b32 v116, s60\n"
    "v_mov_b32 v117, 0\n"
    "buffer_store_dword v116, v117, %[rs], s71 offen sc1\n"
    "s_mov_b64 exec, -1\n"
    "LAfin_%=:\n"
    "s_waitcnt vmcnt(0)\n"

    :: [rs] "s"(rs), [par2] "s"(par2), [w] "s"(w), [nu] "s"(nu), [xb] "s"(P.x), [oex] "i"(O_EX), [spin] "i"(SPIN_ASM), [tag] "i"(TAG),
       [sw] "s"(O_AF + (unsigned)(((h * 14 + u0) * 2) * 14 + k0) * KB),
       [ssm] "s"(32768u + (e >= 40 ? 16384u + (unsigned)(e - 40) * 128u : (unsigned)e * 128u)),
       [ssxm] "s"(O_SXM + (unsigned)(h * 5 + cj) * KB), [sfl] "s"(FL_CA + (unsigned)ci * 256u), [m16] "s"(M16),
       [c0] "i"(PJ_C0), [c1] "i"(PJ_C1), [rr] "i"(PJ_R), [pp] "i"(PJ_P), [r1] "i"(PJ_R1), [pinc] "i"(PJ_PINC), [d1] "i"(PJ_D1), [nd2] "i"(-(PJ_D2)), [pcap] "i"(PJ_PCAP), [pinc2] "i"(PJ_PINC2), [dst] "i"(PJ_DST), [fst] "i"(PJ_FST), [pa0] "i"(PJ_PA0), [mcap] "i"(PJ_MCAP), [psw] "i"(PJ_SW), [pdp] "i"(PJ_DP), [vstp] "v"(STAMP_OFF + (lane >> 4) * 128u),
       [vl16] "v"(lane * 16), [voa] "v"(voa), [vob] "v"(vob), [vcf] "v"(vcf), [vcf2] "v"(vcf2),
       [vx0] "v"(vx0), [vx1] "v"(vx1), [vx20] "v"(vx20), [vx21] "v"(vx21),
       [vredw] "v"(16384u + w * 256u + (unsigned)(g * 8 + n) * 16u), [vredr] "v"(16384u + lane * 16u),
       [vsmo] "v"((lane & 7) * 16u + (lane >> 3) * 8u),
       [vred2w] "v"(17408u + w * 1024u + (unsigned)(g * 16 + n) * 16u), [vred2r] "v"(17408u + lane * 16u),
       [vsxo] "v"((unsigned)(((lane >> 5) * 16 + (lane & 15)) * 8 + 4 * ((lane >> 4) & 1)) * 4u), [vwrm] "v"(vwrm) TS_OPS
    : "v0", "v1", "v2", "v3", "v4", "v5", "v6", "v7", "v8", "v9", "v10", "v11", "v12", "v13", "v14", "v15", "v16", "v17", "v18", "v19", "v20", "v21", "v22", "v23", "v24", "v25", "v26", "v27", "v28", "v29", "v30", "v31", "v32", "v33", "v34", "v35", "v36", "v37", "v38", "v39", "v40", "v41", "v42", "v43", "v44", "v45", "v46", "v47", "v48", "v49", "v50", "v51", "v52", "v53", "v54", "v55", "v56", "v57", "v58", "v59", "v60", "v61", "v62", "v63", "v64", "v65", "v66", "v67", "v68", "v69", "v70", "v71", "v72", "v73", "v74", "v75", "v76", "v77", "v78", "v79", "v80", "v81", "v82", "v83", "v84", "v85", "v86", "v87", "v88", "v89", "v90", "v91", "v92", "v93", "v94", "v95", "v96", "v97", "v98", "v99", "v100", "v101", "v102", "v103", "v104", "v105", "v106", "v107", "v108", "v109", "v110", "v111", "v112", "v113", "v114", "v115", "v116", "v117", "v118", "v119", "v120", "v121", "v122", "v123", "v124", "v125", "v126", "v127", "v128", "v129", "v130", "v131", "v132", "v133", "v134", "v135", "v136", "v137", "v138", "v139", "v140", "v141", "v142", "v143", "a0", "a1", "a2", "a3", "a4", "a5", "a6", "a7", "a8", "a9", "a10", "a11", "a12", "a13", "a14", "a15", "a16", "a17", "a18", "a19", "a20", "a21", "a22", "a23", "a24", "a25", "a26", "a27", "a28", "a29", "a30", "a31", "a32", "a33", "a34", "a35", "a36", "a37", "a38", "a39", "a40", "a41", "a42", "a43", "a44", "a45", "a46", "a47", "a48", "a49", "a50", "a51", "a52", "a53", "a54", "a55", "a56", "a57", "a58", "a59", "a60", "a61", "a62", "a63", "a64", "a65", "a66", "a67", "a68", "a69", "a70", "a71", "a72", "a73", "a74", "a75", "a76", "a77", "a78", "a79", "a80", "a81", "a82", "a83", "a84", "a85", "a86", "a87", "a88", "a89", "a90", "a91", "a92", "a93", "a94", "a95", "s60", "s61", "s62", "s63", "s64", "s65", "s66", "s67", "s68", "s69", "s70", "s71", "s72", "s73", "s74", "s75", "s76", "s77", "s78", "s79", "s80", "s81", "s82", "s83", "s84", "s85", "s86", "s87", "s88", "s89", "s90", "s91", "s92", "s93", "s94", "s95", "s96", "s97", "s98", "s99", "s100", "s101", "vcc", "scc", "memory");
}

DEV void role_X(const Params& P, int h, unsigned par2, int w, int lane) {
    const int n = lane & 15, g = lane >> 4;
    const int k0 = w < 2 ? 4 * w : 8 + 3 * (w - 2), nk = w < 2 ? 4 : 3;
    const int ka = k0 + (n >> 3), kb = (k0 + 2 + (n >> 3) < k0 + nk) ? k0 + 2 + (n >> 3) : k0 + 2;
    const unsigned voa = (unsigned)((4 * ka + g) * 8 + (n & 7)) * 16u, vob = (unsigned)((4 * kb + g) * 8 + (n & 7)) * 16u;
    const u4 rs = mk_rsrc(P.ws);
    asm volatile(
    "s_mov_b32 s60, 0\n"
    "s_mov_b32 s62, %[spin]\n"
    "s_mov_b32 s70, %[sgf]\n"
    "s_mov_b32 s71, %[sfl]\n"
    "v_mov_b32 v24, 0\n"
    "v_mov_b32 v25, 0\n"
    "v_mov_b32 v26, 0\n"
    "v_mov_b32 v27, 0\n"
    "v_mov_b32 v28, 0\n"
    "v_mov_b32 v29, 0\n"
    "v_mov_b32 v30, 0\n"
    "v_mov_b32 v31, 0\n"
    "v_mov_b32 v32, 0\n"
    "v_mov_b32 v33, 0\n"
    "v_mov_b32 v34, 0\n"
    "v_mov_b32 v35, 0\n"
    "v_mov_b32 v36, 0\n"
    "v_mov_b32 v37, 0\n"
    "v_mov_b32 v38, 0\n"
    "v_mov_b32 v39, 0\n"
    "v_mov_b32 v40, 0\n"
    "v_mov_b32 v41, 0\n"
    "v_mov_b32 v42, 0\n"
    "v_mov_b32 v43, 0\n"
    "v_mov_b32 v44, 0\n"
    "v_mov_b32 v45, 0\n"
    "v_mov_b32 v46, 0\n"
    "v_mov_b32 v47, 0\n"
    "v_mov_b32 v48, 0\n"
    "v_mov_b32 v49, 0\n"
    "v_mov_b32 v50, 0\n"
    "v_mov_b32 v51, 0\n"
    "v_mov_b32 v52, 0\n"
    "v_mov_b32 v53, 0\n"
    "v_mov_b32 v54, 0\n"
    "v_mov_b32 v55, 0\n"
    "v_mov_b32 v128, -1\n"
    "v_mov_b32 v129, -1\n"
    "v_mov_b32 v130, -1\n"
    "v_mov_b32 v131, -1\n"
    "s_mov_b32 s83, 0\n"
    "s_mov_b32 s101, 0\n"
    "s_cmp_eq_u32 s101, 0\n"
    "s_cbranch_scc1 LXq0nr_%=\n"
    "s_mov_b32 s101, 0\n"
    "v_readlane_b32 s83, v105, 0\n"
    "v_readlane_b32 s65, v105, 1\n"
    "s_nop 0\n"
    "s_min_u32 s83, s83, s65\n"
    "v_readlane_b32 s65, v105, 2\n"
    "s_nop 0\n"
    "s_min_u32 s83, s83, s65\n"
    "s_nop 0\n"
    "LXq0nr_%=:\n"
    "s_add_u32 s64, s60, 1\n"
    "LXq0r_%=:\n"
    "s_cmp_ge_u32 s83, s64\n"
    "s_cbranch_scc1 LXq0ok_%=\n"
    "s_sub_u32 s62, s62, 1\n"
    "s_cbranch_scc1 LXq0g_%=\n"
    "buffer_load_dword v105, %[vfo], %[rs], s71 offen sc1\n"
    "s_waitcnt vmcnt(0)\n"
    "v_readlane_b32 s83, v105, 0\n"
    "v_readlane_b32 s65, v105, 1\n"
    "s_nop 0\n"
    "s_min_u32 s83, s83, s65\n"
    "v_readlane_b32 s65, v105, 2\n"
    "s_nop 0\n"
    "s_min_u32 s83, s83, s65\n"
    "s_nop 0\n"
    "s_branch LXq0r_%=\n"
    "LXq0g_%=:\n"
    "s_mov_b32 s62, 0\n"
    "LXq0ok_%=:\n"
    "s_add_u32 s64, s70, 0\n"
    "buffer_load_dwordx4 v[24:27], %[vl16], %[rs], s64 offen offset:0 sc1\n"
    "buffer_load_dwordx4 v[28:31], %[vl16], %[rs], s64 offen offset:1024 sc1\n"
    "buffer_load_dwordx4 v[32:35], %[vl16], %[rs], s64 offen offset:2048 sc1\n"
    "s_cmp_lt_u32 %[w], 2\n"
    "s_cbranch_scc0 LXg00_%=\n"
    "buffer_load_dwordx4 v[36:39], %[vl16], %[rs], s64 offen offset:3072 sc1\n"
    "LXg00_%=:\n"
    "s_add_u32 s64, s70, 14336\n"
    "buffer_load_dwordx4 v[40:43], %[vl16], %[rs], s64 offen offset:0 sc1\n"
    "buffer_load_dwordx4 v[44:47], %[vl16], %[rs], s64 offen offset:1024 sc1\n"
    "buffer_load_dwordx4 v[48:51], %[vl16], %[rs], s64 offen offset:2048 sc1\n"
    "s_cmp_lt_u32 %[w], 2\n"
    "s_cbranch_scc0 LXg10_%=\n"
    "buffer_load_dwordx4 v[52:55], %[vl16], %[rs], s64 offen offset:3072 sc1\n"
    "LXg10_%=:\n"
    "s_add_u32 s70, s70, 458752\n"
    "s_lshr_b32 s64, s60, 5\n"
    "s_lshr_b32 s64, %[par2], s64\n"
    "s_and_b32 s64, s64, 1\n"
    "s_lshl_b32 s64, s64, 13\n"
    "s_lshl_b32 s61, s60, 16\n"
    "s_add_u32 s61, s61, s64\n"
    "s_add_u32 s61, s61, %[oex]\n"
    "s_memrealtime s[78:79]\n"
    "s_waitcnt lgkmcnt(0)\n"
    "s_mov_b32 s76, s78\n"
    "s_mov_b32 s98, 0\n"
    "s_mov_b32 s99, %[pa0]\n"
    "LXtop_%=:\n"
    "s_mov_b32 s63, 0\n"
    "s_add_u32 s65, s76, s98\n"
    "s_mov_b32 s80, 2048\n"
    "LXtw_%=:\n"
    "s_memrealtime s[78:79]\n"
    "s_sub_u32 s80, s80, 1\n"
    "s_cbranch_scc1 LXtx_%=\n"
    "s_waitcnt lgkmcnt(0)\n"
    "s_sub_u32 s64, s65, s78\n"
    "s_cmp_gt_i32 s64, 3\n"
    "s_cbranch_scc1 LXtw_%=\n"
    "LXtv_%=:\n"
    "s_cmp_gt_i32 s64, 0\n"
    "s_cbranch_scc0 LXtx_%=\n"
    "s_sub_u32 s64, s64, 1\n"
    "s_nop 1\n"
    "s_branch LXtv_%=\n"
    "LXtx_%=:\n"
    "s_branch LXpmf_%=\n"
    "LXpmr_%=:\n"
    "s_mov_b32 s63, 1\n"
    "s_sub_u32 s62, s62, 1\n"
    "s_cbranch_scc1 LXpmg_%=\n"
    "LXpmf_%=:\n"
    "buffer_load_dwordx4 v[0:3], %[voa], %[rs], s61 offen sc1\n"
    "buffer_load_dwordx4 v[4:7], %[vob], %[rs], s61 offen sc1\n"
    "buffer_load_dword v104, %[vstp], %[rs], s61 offen sc1\n"
    "s_waitcnt vmcnt(0)\n"
    "v_max3_u32 v96, v0, v1, v2\n"
    "v_max3_u32 v97, v3, v4, v5\n"
    "v_max3_u32 v96, v96, v6, v7\n"
    "v_max3_u32 v96, v96, v97, v104\n"
    "v_cmp_eq_u32 vcc, -1, v96\n"
    "s_cbranch_vccnz LXpmr_%=\n"
    "LXpmd_%=:\n"
    "v_mfma_f32_16x16x32_bf16 v[16:19], v[24:27], v[0:3], 0\n"
    "v_mov_b32_dpp v8, v0 row_ror:8 row_mask:0xf bank_mask:0xf\n"
    "v_mov_b32_dpp v9, v1 row_ror:8 row_mask:0xf bank_mask:0xf\n"
    "v_mfma_f32_16x16x32_bf16 v[20:23], v[40:43], v[0:3], 0\n"
    "v_mov_b32_dpp v10, v2 row_ror:8 row_mask:0xf bank_mask:0xf\n"
    "v_mov_b32_dpp v11, v3 row_ror:8 row_mask:0xf bank_mask:0xf\n"
    "v_mfma_f32_16x16x32_bf16 v[16:19], v[32:35], v[4:7], v[16:19]\n"
    "v_mov_b32_dpp v12, v4 row_ror:8 row_mask:0xf bank_mask:0xf\n"
    "v_mov_b32_dpp v13, v5 row_ror:8 row_mask:0xf bank_mask:0xf\n"
    "v_mfma_f32_16x16x32_bf16 v[20:23], v[48:51], v[4:7], v[20:23]\n"
    "v_mov_b32_dpp v14, v6 row_ror:8 row_mask:0xf bank_mask:0xf\n"
    "v_mov_b32_dpp v15, v7 row_ror:8 row_mask:0xf bank_mask:0xf\n"
    "v_mfma_f32_16x16x32_bf16 v[16:19], v[28:31], v[8:11], v[16:19]\n"
    "v_mfma_f32_16x16x32_bf16 v[20:23], v[44:47], v[8:11], v[20:23]\n"
    "v_mfma_f32_16x16x32_bf16 v[16:19], v[36:39], v[12:15], v[16:19]\n"
    "v_mfma_f32_16x16x32_bf16 v[20:23], v[52:55], v[12:15], v[20:23]\n"
    "s_mov_b64 exec, %[m8]\n"
    "s_nop 5\n"
    "ds_write_b128 %[vredw], v[16:19]\n"
    "s_nop 0\n"
    "ds_write_b128 %[vredw], v[20:23] offset:512\n"
    "s_mov_b64 exec, -1\n"
    "s_waitcnt lgkmcnt(0)\n"
    "s_barrier\n"
    "s_cmp_eq_u32 %[w], 0\n"
    "s_cbranch_scc0 LXskip_%=\n"
    "s_mov_b64 exec, %[m8]\n"
    "ds_read_b128 v[56:59], %[vredr] offset:0\n"
    "ds_read_b128 v[60:63], %[vredr] offset:512\n"
    "ds_read_b128 v[64:67], %[vredr] offset:1024\n"
    "ds_read_b128 v[68:71], %[vredr] offset:1536\n"
    "ds_read_b128 v[72:75], %[vredr] offset:2048\n"
    "ds_read_b128 v[76:79], %[vredr] offset:2560\n"
    "ds_read_b128 v[80:83], %[vredr] offset:3072\n"
    "ds_read_b128 v[84:87], %[vredr] offset:3584\n"
    "s_add_u32 s65, s61, 16384\n"
    "s_waitcnt lgkmcnt(4)\n"
    "v_pk_add_f32 v[56:57], v[56:57], v[64:65]\n"
    "v_pk_add_f32 v[58:59], v[58:59], v[66:67]\n"
    "v_pk_add_f32 v[60:61], v[60:61], v[68:69]\n"
    "v_pk_add_f32 v[62:63], v[62:63], v[70:71]\n"
    "s_waitcnt lgkmcnt(0)\n"
    "v_pk_add_f32 v[72:73], v[72:73], v[80:81]\n"
    "v_pk_add_f32 v[74:75], v[74:75], v[82:83]\n"
    "v_pk_add_f32 v[76:77], v[76:77], v[84:85]\n"
    "v_pk_add_f32 v[78:79], v[78:79], v[86:87]\n"
    "v_pk_add_f32 v[56:57], v[56:57], v[72:73]\n"
    "v_pk_add_f32 v[58:59], v[58:59], v[74:75]\n"
    "v_pk_add_f32 v[60:61], v[60:61], v[76:77]\n"
    "v_pk_add_f32 v[62:63], v[62:63], v[78:79]\n"
    "v_max3_f32 v96, v56, v57, v58\n"
    "v_max3_f32 v97, v59, v60, v61\n"
    "v_max3_f32 v96, v96, v62, v63\n"
    "v_max_f32 v96, v96, v97\n"
    "v_mov_b32 v97, v96\n"
    "s_mov_b64 exec, -1\n"
    "s_nop 1\n"
    "v_permlane16_swap_b32 v96, v97\n"
    "v_max_f32 v96, v96, v97\n"
    "v_mov_b32 v97, v96\n"
    "s_nop 1\n"
    "v_permlane32_swap_b32 v96, v97\n"
    "v_max_f32 v96, v96, v97\n"
    "v_sub_f32 v56, v56, v96\n"
    "v_sub_f32 v57, v57, v96\n"
    "v_sub_f32 v58, v58, v96\n"
    "v_sub_f32 v59, v59, v96\n"
    "v_sub_f32 v60, v60, v96\n"
    "v_sub_f32 v61, v61, v96\n"
    "v_sub_f32 v62, v62, v96\n"
    "v_sub_f32 v63, v63, v96\n"
    "v_exp_f32 v56, v56\n"
    "v_exp_f32 v57, v57\n"
    "v_exp_f32 v58, v58\n"
    "v_exp_f32 v59, v59\n"
    "v_exp_f32 v60, v60\n"
    "v_exp_f32 v61, v61\n"
    "v_exp_f32 v62, v62\n"
    "v_exp_f32 v63, v63\n"
    "s_nop 0\n"
    "v_cvt_pk_bf16_f32 v88, v56, v57\n"
    "v_cvt_pk_bf16_f32 v89, v58, v59\n"
    "v_cvt_pk_bf16_f32 v90, v60, v61\n"
    "v_cvt_pk_bf16_f32 v91, v62, v63\n"
    "s_mov_b64 exec, %[m8]\n"
    "buffer_store_dwordx4 v[88:91], %[veo], %[rs], s65 offen sc1\n"
    "s_mov_b64 exec, -1\n"
    "v_pk_add_f32 v[56:57], v[56:57], v[58:59]\n"
    "v_pk_add_f32 v[60:61], v[60:61], v[62:63]\n"
    "v_pk_add_f32 v[56:57], v[56:57], v[60:61]\n"
    "v_add_f32 v97, v56, v57\n"
    "v_mov_b32 v98, v97\n"
    "s_nop 1\n"
    "v_permlane16_swap_b32 v97, v98\n"
    "v_add_f32 v97, v97, v98\n"
    "v_mov_b32 v98, v97\n"
    "s_nop 1\n"
    "v_permlane32_swap_b32 v97, v98\n"
    "v_add_f32 v97, v97, v98\n"
    "s_mov_b64 exec, 0xff\n"
    "buffer_store_dwordx2 v[96:97], %[vsto], %[rs], s61 offen sc1\n"
    "s_mov_b64 exec, -1\n"
    "LXskip_%=:\n"
    "v_readlane_b32 s66, v104, 0\n"
    "v_readlane_b32 s67, v104, 16\n"
    "s_nop 0\n"
    "v_readlane_b32 s80, v104, 32\n"
    "v_readlane_b32 s78, v104, 48\n"
    "s_nop 0\n"
    "s_sub_u32 s66, s66, s76\n"
    "s_sub_u32 s67, s67, s76\n"
    "s_sub_u32 s80, s80, s76\n"
    "s_sub_u32 s78, s78, s76\n"
    "s_nop 0\n"
    "s_max_i32 s66, s66, s67\n"
    "s_max_i32 s80, s80, s78\n"
    "s_max_i32 s66, s66, s80\n"
    "s_nop 0\n"
    "s_add_i32 s66, s66, %[c0]\n"
    "s_add_i32 s67, s66, %[rr]\n"
    "s_add_i32 s80, s66, %[r1]\n"
    "s_cmp_gt_i32 s66, 0\n"
    "s_cselect_b32 s78, %[pinc], 0\n"
    "s_cmp_gt_i32 s66, %[d1]\n"
    "s_cselect_b32 s67, s67, s80\n"
    "s_cselect_b32 s78, %[pinc2], s78\n"
    "s_cmp_ge_u32 s60, %[dst]\n"
    "s_cselect_b32 s80, -1, 0\n"
    "s_cmp_lt_i32 s66, %[nd2]\n"
    "s_cselect_b32 s78, s80, s78\n"
    "s_cmp_le_u32 s60, %[fst]\n"
    "s_cselect_b32 s78, 0, s78\n"
    "s_add_i32 s99, s99, s78\n"
    "s_max_i32 s99, s99, 0\n"
    "s_min_i32 s99, s99, %[pcap]\n"
    "s_cmp_gt_i32 s66, 0\n"
    "s_cselect_b32 s66, s67, 0\n"
    "s_cmp_eq_u32 s60, 1\n"
    "s_cselect_b32 s66, s67, s66\n"
    "s_cmp_eq_u32 s60, 0\n"
    "s_cselect_b32 s66, 0, s66\n"
    "s_cmp_eq_u32 s66, 0\n"
    "s_cselect_b32 s80, s63, 0\n"
    "s_lshl_b32 s80, s80, 2\n"
    "s_add_u32 s98, s98, s80\n"
    "s_min_u32 s98, s98, %[mcap]\n"
    "s_add_u32 s77, s76, s66\n"
    "s_add_u32 s76, s77, %[pp]\n"
    "s_cmp_ge_u32 s60, %[psw]\n"
    "s_cselect_b32 s67, %[pdp], 0\n"
    "s_sub_u32 s76, s76, s67\n"
    "s_add_u32 s76, s76, s99\n"
    "s_nop 0\n"
    "s_cmp_lt_u32 s60, 62\n"
    "s_cbranch_scc0 LXnog_%=\n"
    "s_cmp_eq_u32 s101, 0\n"
    "s_cbranch_scc1 LXq1nr_%=\n"
    "s_mov_b32 s101, 0\n"
    "v_readlane_b32 s83, v105, 0\n"
    "v_readlane_b32 s65, v105, 1\n"
    "s_nop 0\n"
    "s_min_u32 s83, s83, s65\n"
    "v_readlane_b32 s65, v105, 2\n"
    "s_nop 0\n"
    "s_min_u32 s83, s83, s65\n"
    "s_nop 0\n"
    "LXq1nr_%=:\n"
    "s_add_u32 s64, s60, 2\n"
    "LXq1r_%=:\n"
    "s_cmp_ge_u32 s83, s64\n"
    "s_cbranch_scc1 LXq1ok_%=\n"
    "s_sub_u32 s62, s62, 1\n"
    "s_cbranch_scc1 LXq1g_%=\n"
    "buffer_load_dword v105, %[vfo], %[rs], s71 offen sc1\n"
    "s_waitcnt vmcnt(0)\n"
    "v_readlane_b32 s83, v105, 0\n"
    "v_readlane_b32 s65, v105, 1\n"
    "s_nop 0\n"
    "s_min_u32 s83, s83, s65\n"
    "v_readlane_b32 s65, v105, 2\n"
    "s_nop 0\n"
    "s_min_u32 s83, s83, s65\n"
    "s_nop 0\n"
    "s_branch LXq1r_%=\n"
    "LXq1g_%=:\n"
    "s_mov_b32 s62, 0\n"
    "LXq1ok_%=:\n"
    "s_add_u32 s64, s70, 0\n"
    "buffer_load_dwordx4 v[24:27], %[vl16], %[rs], s64 offen offset:0 sc1\n"
    "buffer_load_dwordx4 v[28:31], %[vl16], %[rs], s64 offen offset:1024 sc1\n"
    "buffer_load_dwordx4 v[32:35], %[vl16], %[rs], s64 offen offset:2048 sc1\n"
    "s_cmp_lt_u32 %[w], 2\n"
    "s_cbranch_scc0 LXg01_%=\n"
    "buffer_load_dwordx4 v[36:39], %[vl16], %[rs], s64 offen offset:3072 sc1\n"
    "LXg01_%=:\n"
    "s_add_u32 s64, s70, 14336\n"
    "buffer_load_dwordx4 v[40:43], %[vl16], %[rs], s64 offen offset:0 sc1\n"
    "buffer_load_dwordx4 v[44:47], %[vl16], %[rs], s64 offen offset:1024 sc1\n"
    "buffer_load_dwordx4 v[48:51], %[vl16], %[rs], s64 offen offset:2048 sc1\n"
    "s_cmp_lt_u32 %[w], 2\n"
    "s_cbranch_scc0 LXg11_%=\n"
    "buffer_load_dwordx4 v[52:55], %[vl16], %[rs], s64 offen offset:3072 sc1\n"
    "LXg11_%=:\n"
    "s_add_u32 s70, s70, 458752\n"
    "s_add_u32 s64, s60, 12\n"
    "s_min_u32 s64, s64, 64\n"
    "s_cmp_ge_u32 s83, s64\n"
    "s_cbranch_scc1 LXq2nf_%=\n"
    "buffer_load_dword v105, %[vfo], %[rs], s71 offen sc1\n"
    "s_mov_b32 s101, 1\n"
    "LXq2nf_%=:\n"
    "LXnog_%=:\n"
    "s_add_u32 s60, s60, 1\n"
    "s_cmp_lt_u32 s60, 63\n"
    "s_cbranch_scc0 LXend_%=\n"
    "s_lshr_b32 s64, s60, 5\n"
    "s_lshr_b32 s64, %[par2], s64\n"
    "s_and_b32 s64, s64, 1\n"
    "s_lshl_b32 s64, s64, 13\n"
    "s_lshl_b32 s61, s60, 16\n"
    "s_add_u32 s61, s61, s64\n"
    "s_add_u32 s61, s61, %[oex]\n"
    "s_branch LXtop_%=\n"
    "LXpmg_%=:\n"
    "s_mov_b32 s62, 0\n"
    "s_branch LXpmd_%=\n"
    "LXend_%=:\n"
    "s_waitcnt vmcnt(0)\n"

    :: [rs] "s"(rs), [par2] "s"(par2), [w] "s"(w), [oex] "i"(O_EX), [spin] "i"(SPIN_ASM), [tag] "i"(TAG),
       [sgf] "s"(O_GF + (unsigned)((h * 2) * 14 + k0) * KB), [sfl] "s"(FL_CG), [m8] "s"(M8),
       [c0] "i"(PJ_C0), [c1] "i"(PJ_C1), [rr] "i"(PJ_R), [pp] "i"(PJ_P), [r1] "i"(PJ_R1), [pinc] "i"(PJ_PINC), [d1] "i"(PJ_D1), [nd2] "i"(-(PJ_D2)), [pcap] "i"(PJ_PCAP), [pinc2] "i"(PJ_PINC2), [dst] "i"(PJ_DST), [fst] "i"(PJ_FST), [pa0] "i"(PJ_PA0), [mcap] "i"(PJ_MCAP), [psw] "i"(PJ_SW), [pdp] "i"(PJ_DP), [vstp] "v"(STAMP_OFF + (lane >> 4) * 128u),
       [vl16] "v"(lane * 16), [voa] "v"(voa), [vob] "v"(vob), [vfo] "v"((unsigned)(((14 * h) >> 3) + lane < ((14 * h + 13) >> 3) ? ((14 * h) >> 3) + lane : ((14 * h + 13) >> 3)) * 256u),
       [vredw] "v"(w * 1024u + (unsigned)(g * 8 + (n & 7)) * 16u), [vredr] "v"((unsigned)(g * 8 + (n & 7)) * 16u),
       [veo] "v"((unsigned)(h >> 1) * 1024u + (unsigned)(g * 16 + (h & 1) * 8 + (n & 7)) * 16u),
       [vsto] "v"(49152u + 5120u + (unsigned)h * 128u + (unsigned)(n & 7) * 8u),
       [vtch] "v"(lane < 56 ? lane * 128u : STAMP_OFF + (unsigned)(lane & 3) * 128u) TS_OPS
    : "v0", "v1", "v2", "v3", "v4", "v5", "v6", "v7", "v8", "v9", "v10", "v11", "v12", "v13", "v14", "v15", "v16", "v17", "v18", "v19", "v20", "v21", "v22", "v23", "v24", "v25", "v26", "v27", "v28", "v29", "v30", "v31", "v32", "v33", "v34", "v35", "v36", "v37", "v38", "v39", "v40", "v41", "v42", "v43", "v44", "v45", "v46", "v47", "v48", "v49", "v50", "v51", "v52", "v53", "v54", "v55", "v56", "v57", "v58", "v59", "v60", "v61", "v62", "v63", "v64", "v65", "v66", "v67", "v68", "v69", "v70", "v71", "v72", "v73", "v74", "v75", "v76", "v77", "v78", "v79", "v80", "v81", "v82", "v83", "v84", "v85", "v86", "v87", "v88", "v89", "v90", "v91", "v92", "v93", "v94", "v95", "v96", "v97", "v98", "v99", "v100", "v101", "v102", "v103", "v104", "v105", "v106", "v107", "v108", "v109", "v110", "v111", "v112", "v113", "v114", "v115", "v116", "v117", "v118", "v119", "v120", "v121", "v122", "v123", "v124", "v125", "v126", "v127", "v128", "v129", "v130", "v131", "v132", "v133", "v134", "v135", "v136", "v137", "v138", "v139", "v140", "v141", "v142", "v143", "s60", "s61", "s62", "s63", "s64", "s65", "s66", "s67", "s68", "s69", "s70", "s71", "s72", "s73", "s74", "s75", "s76", "s77", "s78", "s79", "s80", "s81", "s82", "s83", "s84", "s85", "s86", "s87", "s88", "s89", "s90", "s91", "s92", "s93", "s94", "s95", "s96", "s97", "s98", "s99", "s100", "s101", "vcc", "scc", "memory");
}

DEV void role_B(const Params& P, int j, unsigned par2, int w, int lane) {
    const int n = lane & 15, g = lane >> 4;
    const int k0 = w < 2 ? 4 * w : 8 + 3 * (w - 2), nk = w < 2 ? 4 : 3;
    const int ka = k0 + (n >> 3), kb = (k0 + 2 + (n >> 3) < k0 + nk) ? k0 + 2 + (n >> 3) : k0 + 2;
    const unsigned voa = (unsigned)((4 * ka + g) * 8 + (n & 7)) * 16u, vob = (unsigned)((4 * kb + g) * 8 + (n & 7)) * 16u;
    unsigned vs[3];
#pragma unroll
    for (int q = 0; q < 3; ++q) {
        const int cj = q < 2 ? 2 * q + (g >> 1) : 4;
        const int e = (4 * w + 2 * (g & 1) + (n >> 3)) * 5 + cj;
        vs[q] = (e >= 40 ? 16384u + (unsigned)(e - 40) * 128u : (unsigned)e * 128u) + (n & 7) * 16u;
    }
    const int di = (n & 7) - 4 * (g & 1);
    const unsigned m01 = (di == 0 ? 0xFFFFu : 0u) | (di == 1 ? 0xFFFF0000u : 0u);
    const unsigned m23 = (di == 2 ? 0xFFFFu : 0u) | (di == 3 ? 0xFFFF0000u : 0u);
    const u4 rs = mk_rsrc(P.ws);
    asm volatile(
    "s_mov_b32 s60, 0\n"
    "s_mov_b32 s62, %[spin]\n"
    "v_mov_b32 v114, %[voa]\n"
    "v_mov_b32 v115, %[vob]\n"
    "v_mov_b32 v116, %[vlr]\n"
    "s_mov_b32 s70, %[svf]\n"
    "s_mov_b32 s71, %[sflv]\n"
    "s_mov_b32 s72, %[st2]\n"
    "s_mov_b32 s73, %[sflb]\n"
    "s_mov_b32 s74, 0\n"
    "s_mov_b32 s75, -1\n"
    "v_mov_b32 v128, -1\n"
    "v_mov_b32 v129, -1\n"
    "v_mov_b32 v130, -1\n"
    "v_mov_b32 v131, -1\n"
    "v_cmp_eq_u32 vcc, 0, %[vseli]\n"
    "s_nop 1\n"
    "v_cndmask_b32 v118, 0, %[vselv], vcc\n"
    "v_cmp_eq_u32 vcc, 1, %[vseli]\n"
    "s_nop 1\n"
    "v_cndmask_b32 v119, 0, %[vselv], vcc\n"
    "v_cmp_eq_u32 vcc, 2, %[vseli]\n"
    "s_nop 1\n"
    "v_cndmask_b32 v120, 0, %[vselv], vcc\n"
    "v_cmp_eq_u32 vcc, 3, %[vseli]\n"
    "s_nop 1\n"
    "v_cndmask_b32 v121, 0, %[vselv], vcc\n"
    "s_nop 0\n"
    "v_cndmask_b32 v122, v118, 0, s[74:75]\n"
    "v_cndmask_b32 v123, v119, 0, s[74:75]\n"
    "v_cndmask_b32 v124, v120, 0, s[74:75]\n"
    "v_cndmask_b32 v125, v121, 0, s[74:75]\n"
    "s_mov_b32 s64, %[sw]\n"
    "buffer_load_dwordx4 a[0:3], %[vl16], %[rs], s64 offen offset:0\n"
    "buffer_load_dwordx4 a[4:7], %[vl16], %[rs], s64 offen offset:1024\n"
    "buffer_load_dwordx4 a[8:11], %[vl16], %[rs], s64 offen offset:2048\n"
    "buffer_load_dwordx4 a[12:15], %[vl16], %[rs], s64 offen offset:3072\n"
    "s_add_u32 s64, s64, 4096\n"
    "buffer_load_dwordx4 a[16:19], %[vl16], %[rs], s64 offen offset:0\n"
    "buffer_load_dwordx4 a[20:23], %[vl16], %[rs], s64 offen offset:1024\n"
    "buffer_load_dwordx4 a[24:27], %[vl16], %[rs], s64 offen offset:2048\n"
    "buffer_load_dwordx4 a[28:31], %[vl16], %[rs], s64 offen offset:3072\n"
    "s_add_u32 s64, s64, 4096\n"
    "buffer_load_dwordx4 a[32:35], %[vl16], %[rs], s64 offen offset:0\n"
    "buffer_load_dwordx4 a[36:39], %[vl16], %[rs], s64 offen offset:1024\n"
    "buffer_load_dwordx4 a[40:43], %[vl16], %[rs], s64 offen offset:2048\n"
    "buffer_load_dwordx4 a[44:47], %[vl16], %[rs], s64 offen offset:3072\n"
    "s_add_u32 s64, s64, 4096\n"
    "buffer_load_dwordx4 a[48:51], %[vl16], %[rs], s64 offen offset:0\n"
    "buffer_load_dwordx4 a[52:55], %[vl16], %[rs], s64 offen offset:1024\n"
    "s_add_u32 s64, s64, 389120\n"
    "buffer_load_dwordx4 a[56:59], %[vl16], %[rs], s64 offen offset:0\n"
    "buffer_load_dwordx4 a[60:63], %[vl16], %[rs], s64 offen offset:1024\n"
    "buffer_load_dwordx4 a[64:67], %[vl16], %[rs], s64 offen offset:2048\n"
    "buffer_load_dwordx4 a[68:71], %[vl16], %[rs], s64 offen offset:3072\n"
    "s_add_u32 s64, s64, 4096\n"
    "buffer_load_dwordx4 a[72:75], %[vl16], %[rs], s64 offen offset:0\n"
    "buffer_load_dwordx4 a[76:79], %[vl16], %[rs], s64 offen offset:1024\n"
    "buffer_load_dwordx4 a[80:83], %[vl16], %[rs], s64 offen offset:2048\n"
    "buffer_load_dwordx4 a[84:87], %[vl16], %[rs], s64 offen offset:3072\n"
    "s_add_u32 s64, s64, 4096\n"
    "buffer_load_dwordx4 a[88:91], %[vl16], %[rs], s64 offen offset:0\n"
    "buffer_load_dwordx4 a[92:95], %[vl16], %[rs], s64 offen offset:1024\n"
    "buffer_load_dwordx4 a[96:99], %[vl16], %[rs], s64 offen offset:2048\n"
    "buffer_load_dwordx4 a[100:103], %[vl16], %[rs], s64 offen offset:3072\n"
    "s_add_u32 s64, s64, 4096\n"
    "buffer_load_dwordx4 a[104:107], %[vl16], %[rs], s64 offen offset:0\n"
    "buffer_load_dwordx4 a[108:111], %[vl16], %[rs], s64 offen offset:1024\n"
    "s_add_u32 s64, s64, 389120\n"
    "s_waitcnt vmcnt(20)\n"
    "buffer_load_dwordx4 a[112:115], %[vl16], %[rs], s64 offen offset:0\n"
    "buffer_load_dwordx4 a[116:119], %[vl16], %[rs], s64 offen offset:1024\n"
    "buffer_load_dwordx4 a[120:123], %[vl16], %[rs], s64 offen offset:2048\n"
    "buffer_load_dwordx4 a[124:127], %[vl16], %[rs], s64 offen offset:3072\n"
    "s_add_u32 s64, s64, 4096\n"
    "buffer_load_dwordx4 a[128:131], %[vl16], %[rs], s64 offen offset:0\n"
    "buffer_load_dwordx4 a[132:135], %[vl16], %[rs], s64 offen offset:1024\n"
    "buffer_load_dwordx4 a[136:139], %[vl16], %[rs], s64 offen offset:2048\n"
    "buffer_load_dwordx4 a[140:143], %[vl16], %[rs], s64 offen offset:3072\n"
    "s_add_u32 s64, s64, 4096\n"
    "buffer_load_dwordx4 a[144:147], %[vl16], %[rs], s64 offen offset:0\n"
    "buffer_load_dwordx4 a[148:151], %[vl16], %[rs], s64 offen offset:1024\n"
    "buffer_load_dwordx4 a[152:155], %[vl16], %[rs], s64 offen offset:2048\n"
    "buffer_load_dwordx4 a[156:159], %[vl16], %[rs], s64 offen offset:3072\n"
    "s_add_u32 s64, s64, 4096\n"
    "buffer_load_dwordx4 a[160:163], %[vl16], %[rs], s64 offen offset:0\n"
    "buffer_load_dwordx4 a[164:167], %[vl16], %[rs], s64 offen offset:1024\n"
    "s_add_u32 s64, s64, 389120\n"
    "s_waitcnt vmcnt(20)\n"
    "buffer_load_dwordx4 a[168:171], %[vl16], %[rs], s64 offen offset:0\n"
    "buffer_load_dwordx4 a[172:175], %[vl16], %[rs], s64 offen offset:1024\n"
    "buffer_load_dwordx4 a[176:179], %[vl16], %[rs], s64 offen offset:2048\n"
    "buffer_load_dwordx4 a[180:183], %[vl16], %[rs], s64 offen offset:3072\n"
    "s_add_u32 s64, s64, 4096\n"
    "buffer_load_dwordx4 a[184:187], %[vl16], %[rs], s64 offen offset:0\n"
    "buffer_load_dwordx4 a[188:191], %[vl16], %[rs], s64 offen offset:1024\n"
    "buffer_load_dwordx4 a[192:195], %[vl16], %[rs], s64 offen offset:2048\n"
    "buffer_load_dwordx4 a[196:199], %[vl16], %[rs], s64 offen offset:3072\n"
    "s_add_u32 s64, s64, 4096\n"
    "buffer_load_dwordx4 a[200:203], %[vl16], %[rs], s64 offen offset:0\n"
    "buffer_load_dwordx4 a[204:207], %[vl16], %[rs], s64 offen offset:1024\n"
    "buffer_load_dwordx4 a[208:211], %[vl16], %[rs], s64 offen offset:2048\n"
    "buffer_load_dwordx4 a[212:215], %[vl16], %[rs], s64 offen offset:3072\n"
    "s_add_u32 s64, s64, 4096\n"
    "buffer_load_dwordx4 a[216:219], %[vl16], %[rs], s64 offen offset:0\n"
    "buffer_load_dwordx4 a[220:223], %[vl16], %[rs], s64 offen offset:1024\n"
    "s_add_u32 s64, s64, 389120\n"
    "s_mov_b32 s83, 0\n"
    "s_mov_b32 s101, 0\n"
    "s_cmp_eq_u32 s101, 0\n"
    "s_cbranch_scc1 LBq0nr_%=\n"
    "s_mov_b32 s101, 0\n"
    "v_readlane_b32 s83, v126, 0\n"
    "s_nop 0\n"
    "LBq0nr_%=:\n"
    "s_add_u32 s64, s60, 1\n"
    "LBq0r_%=:\n"
    "s_cmp_ge_u32 s83, s64\n"
    "s_cbranch_scc1 LBq0ok_%=\n"
    "s_sub_u32 s62, s62, 1\n"
    "s_cbranch_scc1 LBq0g_%=\n"
    "buffer_load_dword v126, %[vfo], %[rs], s71 offen sc1\n"
    "s_waitcnt vmcnt(0)\n"
    "v_readlane_b32 s83, v126, 0\n"
    "s_nop 0\n"
    "s_branch LBq0r_%=\n"
    "LBq0g_%=:\n"
    "s_mov_b32 s62, 0\n"
    "LBq0ok_%=:\n"
    "buffer_load_dwordx4 v[88:91], %[vl16], %[rs], s70 offen offset:0 sc1\n"
    "buffer_load_dwordx4 v[92:95], %[vl16], %[rs], s70 offen offset:1024 sc1\n"
    "buffer_load_dwordx4 v[96:99], %[vl16], %[rs], s70 offen offset:2048 sc1\n"
    "buffer_load_dwordx4 v[100:103], %[vl16], %[rs], s70 offen offset:3072 sc1\n"
    "s_add_u32 s70, s70, 458752\n"
    "s_lshr_b32 s64, s60, 5\n"
    "s_lshr_b32 s64, %[par2], s64\n"
    "s_and_b32 s64, s64, 1\n"
    "s_lshl_b32 s64, s64, 13\n"
    "s_lshl_b32 s61, s60, 16\n"
    "s_add_u32 s61, s61, s64\n"
    "s_add_u32 s61, s61, %[oex]\n"
    "s_memrealtime s[78:79]\n"
    "s_waitcnt lgkmcnt(0)\n"
    "s_mov_b32 s76, s78\n"
    "s_mov_b32 s98, 0\n"
    "s_mov_b32 s68, 0\n"
    "s_mov_b32 s69, %[pa0]\n"
    "s_mov_b32 s63, 0\n"
    "s_mov_b32 s81, 0\n"
    "LBtop_%=:\n"
    "s_cmp_eq_u32 s63, 0\n"
    "s_cselect_b32 s64, s81, 0\n"
    "s_cmp_lg_u32 s64, 0\n"
    "s_cselect_b32 s64, %[xinc], 0\n"
    "s_add_u32 s68, s68, s64\n"
    "s_min_u32 s68, s68, %[xcap]\n"
    "s_mov_b32 s63, 0\n"
    "s_mov_b32 s81, 0\n"
    "s_add_u32 s65, s76, s98\n"
    "s_mov_b32 s80, 2048\n"
    "LBtw_%=:\n"
    "s_memrealtime s[78:79]\n"
    "s_sub_u32 s80, s80, 1\n"
    "s_cbranch_scc1 LBtx_%=\n"
    "s_waitcnt lgkmcnt(0)\n"
    "s_sub_u32 s64, s65, s78\n"
    "s_cmp_gt_i32 s64, 3\n"
    "s_cbranch_scc1 LBtw_%=\n"
    "LBtv_%=:\n"
    "s_cmp_gt_i32 s64, 0\n"
    "s_cbranch_scc0 LBtx_%=\n"
    "s_sub_u32 s64, s64, 1\n"
    "s_nop 1\n"
    "s_branch LBtv_%=\n"
    "LBtx_%=:\n"
    "s_branch LBpmf_%=\n"
    "LBpmr_%=:\n"
    "s_mov_b32 s63, 1\n"
    "s_sub_u32 s62, s62, 1\n"
    "s_cbranch_scc1 LBpmg_%=\n"
    "LBpmf_%=:\n"
    "buffer_load_dwordx4 v[56:59], %[voa], %[rs], s61 offen sc1\n"
    "buffer_load_dwordx4 v[60:63], %[vob], %[rs], s61 offen sc1\n"
    "buffer_load_dword v117, %[vstp], %[rs], s61 offen sc1\n"
    "s_waitcnt vmcnt(0)\n"
    "v_max3_u32 v16, v56, v57, v58\n"
    "v_max3_u32 v17, v59, v60, v61\n"
    "v_max3_u32 v16, v16, v62, v63\n"
    "v_max3_u32 v16, v16, v17, v117\n"
    "v_cmp_eq_u32 vcc, -1, v16\n"
    "s_cbranch_vccnz LBpmr_%=\n"
    "LBpmd_%=:\n"
    "ds_write_b128 v114, v[56:59]\n"
    "ds_write_b128 v115, v[60:63]\n"
    "s_waitcnt lgkmcnt(0)\n"
    "s_barrier\n"
    "ds_read_b128 v[0:3], v116 offset:0\n"
    "ds_read_b128 v[4:7], v116 offset:512\n"
    "ds_read_b128 v[8:11], v116 offset:1024\n"
    "ds_read_b128 v[12:15], v116 offset:1536\n"
    "ds_read_b128 v[16:19], v116 offset:2048\n"
    "ds_read_b128 v[20:23], v116 offset:2560\n"
    "ds_read_b128 v[24:27], v116 offset:3072\n"
    "ds_read_b128 v[28:31], v116 offset:3584\n"
    "ds_read_b128 v[32:35], v116 offset:4096\n"
    "ds_read_b128 v[36:39], v116 offset:4608\n"
    "ds_read_b128 v[40:43], v116 offset:5120\n"
    "ds_read_b128 v[44:47], v116 offset:5632\n"
    "ds_read_b128 v[48:51], v116 offset:6144\n"
    "ds_read_b128 v[52:55], v116 offset:6656\n"
    "s_cmp_eq_u32 s60, 63\n"
    "s_cbranch_scc0 LBe63_%=\n"
    "s_cmp_eq_u32 %[w], 0\n"
    "s_cbranch_scc0 LBe63_%=\n"
    "s_mov_b64 exec, 1\n"
    "v_mov_b32 v132, s60\n"
    "v_mov_b32 v133, 0\n"
    "buffer_store_dword v132, v133, %[rs], s73 offen sc1\n"
    "s_mov_b64 exec, -1\n"
    "LBe63_%=:\n"
    "s_waitcnt lgkmcnt(13)\n"
    "v_mfma_f32_16x16x32_bf16 v[64:67], v[0:3], a[0:3], 0\n"
    "v_readlane_b32 s66, v117, 0\n"
    "v_readlane_b32 s67, v117, 16\n"
    "v_mfma_f32_16x16x32_bf16 v[68:71], v[0:3], a[56:59], 0\n"
    "v_readlane_b32 s80, v117, 32\n"
    "v_readlane_b32 s78, v117, 48\n"
    "v_mfma_f32_16x16x32_bf16 v[72:75], v[0:3], a[112:115], 0\n"
    "s_sub_u32 s66, s66, s76\n"
    "s_sub_u32 s67, s67, s76\n"
    "s_sub_u32 s80, s80, s76\n"
    "s_sub_u32 s78, s78, s76\n"
    "v_mfma_f32_16x16x32_bf16 v[76:79], v[0:3], a[168:171], 0\n"
    "s_max_i32 s66, s66, s67\n"
    "s_max_i32 s80, s80, s78\n"
    "s_max_i32 s66, s66, s80\n"
    "s_waitcnt lgkmcnt(12)\n"
    "v_mfma_f32_16x16x32_bf16 v[64:67], v[4:7], a[4:7], v[64:67]\n"
    "s_add_i32 s66, s66, %[c0]\n"
    "s_add_i32 s67, s66, %[rr]\n"
    "s_add_i32 s80, s66, %[r1]\n"
    "s_cmp_gt_i32 s66, 0\n"
    "s_cselect_b32 s78, %[pinc], 0\n"
    "s_cmp_gt_i32 s66, %[d1]\n"
    "s_cselect_b32 s67, s67, s80\n"
    "s_cselect_b32 s78, %[pinc2], s78\n"
    "s_cmp_ge_u32 s60, %[dst]\n"
    "s_cselect_b32 s80, -1, 0\n"
    "s_cmp_lt_i32 s66, %[nd2]\n"
    "s_cselect_b32 s78, s80, s78\n"
    "s_cmp_le_u32 s60, %[fst]\n"
    "s_cselect_b32 s78, 0, s78\n"
    "s_add_i32 s69, s69, s78\n"
    "s_max_i32 s69, s69, 0\n"
    "s_min_i32 s69, s69, %[pcap]\n"
    "s_cmp_gt_i32 s66, 0\n"
    "s_cselect_b32 s66, s67, 0\n"
    "s_cmp_eq_u32 s60, 1\n"
    "s_cselect_b32 s66, s67, s66\n"
    "s_cmp_eq_u32 s60, 0\n"
    "s_cselect_b32 s66, 0, s66\n"
    "s_cmp_eq_u32 s66, 0\n"
    "s_cselect_b32 s80, s63, 0\n"
    "s_lshl_b32 s80, s80, 2\n"
    "s_add_u32 s98, s98, s80\n"
    "s_min_u32 s98, s98, %[mcap]\n"
    "s_add_u32 s77, s76, s66\n"
    "s_add_u32 s76, s77, %[pp]\n"
    "s_cmp_ge_u32 s60, %[psw]\n"
    "s_cselect_b32 s67, %[pdp], 0\n"
    "s_sub_u32 s76, s76, s67\n"
    "s_add_u32 s76, s76, s69\n"
    "v_mfma_f32_16x16x32_bf16 v[68:71], v[4:7], a[60:63], v[68:71]\n"
    "v_mfma_f32_16x16x32_bf16 v[72:75], v[4:7], a[116:119], v[72:75]\n"
    "v_mfma_f32_16x16x32_bf16 v[76:79], v[4:7], a[172:175], v[76:79]\n"
    "s_waitcnt lgkmcnt(11)\n"
    "v_mfma_f32_16x16x32_bf16 v[64:67], v[8:11], a[8:11], v[64:67]\n"
    "s_add_u32 s99, s77, %[qe]\n"
    "v_mfma_f32_16x16x32_bf16 v[68:71], v[8:11], a[64:67], v[68:71]\n"
    "s_add_u32 s100, s77, %[qd]\n"
    "v_mfma_f32_16x16x32_bf16 v[72:75], v[8:11], a[120:123], v[72:75]\n"
    "s_add_u32 s82, s61, 16384\n"
    "v_mfma_f32_16x16x32_bf16 v[76:79], v[8:11], a[176:179], v[76:79]\n"
    "s_add_u32 s67, s61, 32768\n"
    "s_waitcnt lgkmcnt(10)\n"
    "v_mfma_f32_16x16x32_bf16 v[64:67], v[12:15], a[12:15], v[64:67]\n"
    "s_add_u32 s99, s99, s68\n"
    "v_mfma_f32_16x16x32_bf16 v[68:71], v[12:15], a[68:71], v[68:71]\n"
    "s_add_u32 s100, s100, s68\n"
    "v_mfma_f32_16x16x32_bf16 v[72:75], v[12:15], a[124:127], v[72:75]\n"
    "v_mfma_f32_16x16x32_bf16 v[76:79], v[12:15], a[180:183], v[76:79]\n"
    "s_waitcnt lgkmcnt(9)\n"
    "v_mfma_f32_16x16x32_bf16 v[64:67], v[16:19], a[16:19], v[64:67]\n"
    "v_mfma_f32_16x16x32_bf16 v[68:71], v[16:19], a[72:75], v[68:71]\n"
    "v_mfma_f32_16x16x32_bf16 v[72:75], v[16:19], a[128:131], v[72:75]\n"
    "v_mfma_f32_16x16x32_bf16 v[76:79], v[16:19], a[184:187], v[76:79]\n"
    "s_waitcnt lgkmcnt(8)\n"
    "v_mfma_f32_16x16x32_bf16 v[64:67], v[20:23], a[20:23], v[64:67]\n"
    "v_mfma_f32_16x16x32_bf16 v[68:71], v[20:23], a[76:79], v[68:71]\n"
    "v_mfma_f32_16x16x32_bf16 v[72:75], v[20:23], a[132:135], v[72:75]\n"
    "v_mfma_f32_16x16x32_bf16 v[76:79], v[20:23], a[188:191], v[76:79]\n"
    "s_waitcnt lgkmcnt(7)\n"
    "v_mfma_f32_16x16x32_bf16 v[64:67], v[24:27], a[24:27], v[64:67]\n"
    "v_mfma_f32_16x16x32_bf16 v[68:71], v[24:27], a[80:83], v[68:71]\n"
    "v_mfma_f32_16x16x32_bf16 v[72:75], v[24:27], a[136:139], v[72:75]\n"
    "v_mfma_f32_16x16x32_bf16 v[76:79], v[24:27], a[192:195], v[76:79]\n"
    "s_waitcnt lgkmcnt(6)\n"
    "v_mfma_f32_16x16x32_bf16 v[64:67], v[28:31], a[28:31], v[64:67]\n"
    "v_mfma_f32_16x16x32_bf16 v[68:71], v[28:31], a[84:87], v[68:71]\n"
    "v_mfma_f32_16x16x32_bf16 v[72:75], v[28:31], a[140:143], v[72:75]\n"
    "v_mfma_f32_16x16x32_bf16 v[76:79], v[28:31], a[196:199], v[76:79]\n"
    "s_waitcnt lgkmcnt(5)\n"
    "v_mfma_f32_16x16x32_bf16 v[64:67], v[32:35], a[32:35], v[64:67]\n"
    "v_mfma_f32_16x16x32_bf16 v[68:71], v[32:35], a[88:91], v[68:71]\n"
    "v_mfma_f32_16x16x32_bf16 v[72:75], v[32:35], a[144:147], v[72:75]\n"
    "v_mfma_f32_16x16x32_bf16 v[76:79], v[32:35], a[200:203], v[76:79]\n"
    "s_waitcnt lgkmcnt(4)\n"
    "v_mfma_f32_16x16x32_bf16 v[64:67], v[36:39], a[36:39], v[64:67]\n"
    "v_mfma_f32_16x16x32_bf16 v[68:71], v[36:39], a[92:95], v[68:71]\n"
    "v_mfma_f32_16x16x32_bf16 v[72:75], v[36:39], a[148:151], v[72:75]\n"
    "v_mfma_f32_16x16x32_bf16 v[76:79], v[36:39], a[204:207], v[76:79]\n"
    "s_waitcnt lgkmcnt(3)\n"
    "v_mfma_f32_16x16x32_bf16 v[64:67], v[40:43], a[40:43], v[64:67]\n"
    "v_mfma_f32_16x16x32_bf16 v[68:71], v[40:43], a[96:99], v[68:71]\n"
    "v_mfma_f32_16x16x32_bf16 v[72:75], v[40:43], a[152:155], v[72:75]\n"
    "v_mfma_f32_16x16x32_bf16 v[76:79], v[40:43], a[208:211], v[76:79]\n"
    "s_waitcnt lgkmcnt(2)\n"
    "v_mfma_f32_16x16x32_bf16 v[64:67], v[44:47], a[44:47], v[64:67]\n"
    "v_mfma_f32_16x16x32_bf16 v[68:71], v[44:47], a[100:103], v[68:71]\n"
    "v_mfma_f32_16x16x32_bf16 v[72:75], v[44:47], a[156:159], v[72:75]\n"
    "v_mfma_f32_16x16x32_bf16 v[76:79], v[44:47], a[212:215], v[76:79]\n"
    "s_waitcnt lgkmcnt(1)\n"
    "v_mfma_f32_16x16x32_bf16 v[64:67], v[48:51], a[48:51], v[64:67]\n"
    "v_mfma_f32_16x16x32_bf16 v[68:71], v[48:51], a[104:107], v[68:71]\n"
    "v_mfma_f32_16x16x32_bf16 v[72:75], v[48:51], a[160:163], v[72:75]\n"
    "v_mfma_f32_16x16x32_bf16 v[76:79], v[48:51], a[216:219], v[76:79]\n"
    "s_waitcnt lgkmcnt(0)\n"
    "v_mfma_f32_16x16x32_bf16 v[64:67], v[52:55], a[52:55], v[64:67]\n"
    "v_mfma_f32_16x16x32_bf16 v[68:71], v[52:55], a[108:111], v[68:71]\n"
    "v_mfma_f32_16x16x32_bf16 v[72:75], v[52:55], a[164:167], v[72:75]\n"
    "v_mfma_f32_16x16x32_bf16 v[76:79], v[52:55], a[220:223], v[76:79]\n"
    "s_cmp_eq_u32 s60, 63\n"
    "s_cbranch_scc1 LBl2_%=\n"
    "s_mov_b32 s80, 2048\n"
    "LBew_%=:\n"
    "s_memrealtime s[78:79]\n"
    "s_sub_u32 s80, s80, 1\n"
    "s_cbranch_scc1 LBex_%=\n"
    "s_waitcnt lgkmcnt(0)\n"
    "s_sub_u32 s64, s99, s78\n"
    "s_cmp_gt_i32 s64, 3\n"
    "s_cbranch_scc1 LBew_%=\n"
    "LBev_%=:\n"
    "s_cmp_gt_i32 s64, 0\n"
    "s_cbranch_scc0 LBex_%=\n"
    "s_sub_u32 s64, s64, 1\n"
    "s_nop 1\n"
    "s_branch LBev_%=\n"
    "LBex_%=:\n"
    "buffer_load_dwordx4 v[104:107], %[veo], %[rs], s82 offen sc1\n"
    "buffer_load_dwordx4 v[108:111], %[veo], %[rs], s82 offen offset:1024 sc1\n"
    "buffer_load_dwordx2 v[112:113], %[vsto], %[rs], s61 offen sc1\n"
    "s_mov_b32 s80, 2048\n"
    "LBsw_%=:\n"
    "s_memrealtime s[78:79]\n"
    "s_sub_u32 s80, s80, 1\n"
    "s_cbranch_scc1 LBsx_%=\n"
    "s_waitcnt lgkmcnt(0)\n"
    "s_sub_u32 s64, s100, s78\n"
    "s_cmp_gt_i32 s64, 3\n"
    "s_cbranch_scc1 LBsw_%=\n"
    "LBsv_%=:\n"
    "s_cmp_gt_i32 s64, 0\n"
    "s_cbranch_scc0 LBsx_%=\n"
    "s_sub_u32 s64, s64, 1\n"
    "s_nop 1\n"
    "s_branch LBsv_%=\n"
    "LBsx_%=:\n"
    "buffer_load_dwordx4 v[0:3], %[vs0], %[rs], s67 offen sc1\n"
    "buffer_load_dwordx4 v[4:7], %[vs1], %[rs], s67 offen sc1\n"
    "buffer_load_dwordx4 v[8:11], %[vs2], %[rs], s67 offen sc1\n"
    "LBl2_%=:\n"
    "v_xor_b32 v114, 0x2000, v114\n"
    "v_xor_b32 v115, 0x2000, v115\n"
    "v_xor_b32 v116, 0x2000, v116\n"
    "s_nop 5\n"
    "v_cvt_pk_bf16_f32 v80, v64, v65\n"
    "v_cvt_pk_bf16_f32 v81, v66, v67\n"
    "v_cvt_pk_bf16_f32 v82, v68, v69\n"
    "v_cvt_pk_bf16_f32 v83, v70, v71\n"
    "v_cvt_pk_bf16_f32 v84, v72, v73\n"
    "v_cvt_pk_bf16_f32 v85, v74, v75\n"
    "v_cvt_pk_bf16_f32 v86, v76, v77\n"
    "v_cvt_pk_bf16_f32 v87, v78, v79\n"
    "s_mov_b32 exec_hi, 0\n"
    "buffer_store_dwordx2 v[80:81], %[vt2o], %[rs], s72 offen offset:0 sc1\n"
    "buffer_store_dwordx2 v[82:83], %[vt2o], %[rs], s72 offen offset:256 sc1\n"
    "buffer_store_dwordx2 v[84:85], %[vt2o], %[rs], s72 offen offset:512 sc1\n"
    "buffer_store_dwordx2 v[86:87], %[vt2o], %[rs], s72 offen offset:768 sc1\n"
    "s_mov_b64 exec, -1\n"
    "s_add_u32 s72, s72, 114688\n"
    "s_cmp_eq_u32 s60, 63\n"
    "s_cbranch_scc1 LBlast_%=\n"
    "s_waitcnt vmcnt(7)\n"
    "s_branch LBpec_%=\n"
    "LBper_%=:\n"
    "s_mov_b32 s81, 1\n"
    "s_sub_u32 s62, s62, 1\n"
    "s_cbranch_scc1 LBpeg_%=\n"
    "buffer_load_dwordx4 v[104:107], %[veo], %[rs], s82 offen sc1\n"
    "buffer_load_dwordx4 v[108:111], %[veo], %[rs], s82 offen offset:1024 sc1\n"
    "buffer_load_dwordx2 v[112:113], %[vsto], %[rs], s61 offen sc1\n"
    "s_waitcnt vmcnt(0)\n"
    "LBpec_%=:\n"
    "v_max3_u32 v16, v104, v105, v106\n"
    "v_max3_u32 v17, v107, v108, v109\n"
    "v_max3_u32 v16, v16, v110, v111\n"
    "v_max3_u32 v17, v17, v112, v113\n"
    "v_max_u32 v16, v16, v17\n"
    "v_cmp_eq_u32 vcc, -1, v16\n"
    "s_cbranch_vccnz LBper_%=\n"
    "LBped_%=:\n"
    "v_mfma_f32_16x16x32_bf16 v[64:67], v[104:107], v[88:91], 0\n"
    "v_mfma_f32_16x16x32_bf16 v[68:71], v[104:107], v[92:95], 0\n"
    "v_mfma_f32_16x16x32_bf16 v[72:75], v[108:111], v[96:99], 0\n"
    "v_mfma_f32_16x16x32_bf16 v[76:79], v[108:111], v[100:103], 0\n"
    "s_cmp_eq_u32 s101, 0\n"
    "s_cbranch_scc1 LBq1nr_%=\n"
    "s_mov_b32 s101, 0\n"
    "v_readlane_b32 s83, v126, 0\n"
    "s_nop 0\n"
    "LBq1nr_%=:\n"
    "s_add_u32 s64, s60, 2\n"
    "LBq1r_%=:\n"
    "s_cmp_ge_u32 s83, s64\n"
    "s_cbranch_scc1 LBq1ok_%=\n"
    "s_sub_u32 s62, s62, 1\n"
    "s_cbranch_scc1 LBq1g_%=\n"
    "buffer_load_dword v126, %[vfo], %[rs], s71 offen sc1\n"
    "s_waitcnt vmcnt(0)\n"
    "v_readlane_b32 s83, v126, 0\n"
    "s_nop 0\n"
    "s_branch LBq1r_%=\n"
    "LBq1g_%=:\n"
    "s_mov_b32 s62, 0\n"
    "LBq1ok_%=:\n"
    "buffer_load_dwordx4 v[88:91], %[vl16], %[rs], s70 offen offset:0 sc1\n"
    "buffer_load_dwordx4 v[92:95], %[vl16], %[rs], s70 offen offset:1024 sc1\n"
    "buffer_load_dwordx4 v[96:99], %[vl16], %[rs], s70 offen offset:2048 sc1\n"
    "buffer_load_dwordx4 v[100:103], %[vl16], %[rs], s70 offen offset:3072 sc1\n"
    "s_add_u32 s70, s70, 458752\n"
    "v_cvt_pk_bf16_f32 v24, v64, v65\n"
    "v_cvt_pk_bf16_f32 v25, v66, v67\n"
    "v_cvt_pk_bf16_f32 v26, v68, v69\n"
    "v_cvt_pk_bf16_f32 v27, v70, v71\n"
    "v_cvt_pk_bf16_f32 v28, v72, v73\n"
    "v_cvt_pk_bf16_f32 v29, v74, v75\n"
    "v_cvt_pk_bf16_f32 v30, v76, v77\n"
    "v_cvt_pk_bf16_f32 v31, v78, v79\n"
    "s_nop 1\n"
    "v_permlane32_swap_b32 v80, v24\n"
    "v_permlane32_swap_b32 v81, v25\n"
    "v_permlane32_swap_b32 v84, v28\n"
    "v_permlane32_swap_b32 v85, v29\n"
    "v_cndmask_b32 v82, v82, v26, s[74:75]\n"
    "v_cndmask_b32 v83, v83, v27, s[74:75]\n"
    "v_cndmask_b32 v86, v86, v30, s[74:75]\n"
    "v_cndmask_b32 v87, v87, v31, s[74:75]\n"
    "s_waitcnt vmcnt(8)\n"
    "s_branch LBpsw_%=\n"
    "LBpsr_%=:\n"
    "s_or_b32 s81, s81, 2\n"
    "s_sub_u32 s62, s62, 1\n"
    "s_cbranch_scc1 LBpsg_%=\n"
    "buffer_load_dwordx4 v[0:3], %[vs0], %[rs], s67 offen sc1\n"
    "buffer_load_dwordx4 v[4:7], %[vs1], %[rs], s67 offen sc1\n"
    "buffer_load_dwordx4 v[8:11], %[vs2], %[rs], s67 offen sc1\n"
    "s_waitcnt vmcnt(0)\n"
    "LBpsw_%=:\n"
    "s_memrealtime s[84:85]\n"
    "v_mfma_f32_16x16x32_bf16 v[12:15], v[118:121], v[0:3], 0\n"
    "v_max3_u32 v16, v0, v1, v2\n"
    "v_max3_u32 v17, v3, v4, v5\n"
    "v_mfma_f32_16x16x32_bf16 v[12:15], v[118:121], v[4:7], v[12:15]\n"
    "v_max3_u32 v16, v16, v6, v7\n"
    "v_max3_u32 v17, v17, v8, v9\n"
    "v_mfma_f32_16x16x32_bf16 v[12:15], v[122:125], v[8:11], v[12:15]\n"
    "v_max3_u32 v16, v16, v10, v11\n"
    "v_max_u32 v16, v16, v17\n"
    "v_cmp_eq_u32 vcc, -1, v16\n"
    "s_cbranch_vccnz LBpsr_%=\n"
    "LBpsd_%=:\n"
    "s_nop 3\n"
    "v_max3_f32 v16, v12, v13, v14\n"
    "v_max3_f32 v16, v16, v15, v112\n"
    "v_mov_b32 v17, v16\n"
    "s_nop 1\n"
    "v_permlane16_swap_b32 v16, v17\n"
    "v_max_f32 v16, v16, v17\n"
    "v_sub_f32 v20, v12, v16\n"
    "v_sub_f32 v21, v13, v16\n"
    "v_sub_f32 v22, v14, v16\n"
    "v_sub_f32 v23, v15, v16\n"
    "v_sub_f32 v18, v112, v16\n"
    "v_exp_f32 v20, v20\n"
    "v_exp_f32 v21, v21\n"
    "v_exp_f32 v22, v22\n"
    "v_exp_f32 v23, v23\n"
    "v_exp_f32 v18, v18\n"
    "v_add_f32 v19, v20, v21\n"
    "v_add_f32 v17, v22, v23\n"
    "v_add_f32 v19, v19, v17\n"
    "v_mov_b32 v17, v19\n"
    "s_nop 1\n"
    "v_permlane16_swap_b32 v19, v17\n"
    "v_add_f32 v19, v19, v17\n"
    "v_fma_f32 v19, v113, v18, v19\n"
    "v_rcp_f32 v24, v19\n"
    "s_nop 0\n"
    "v_mul_f32 v30, v18, v24\n"
    "v_pk_mul_f32 v[20:21], v[20:21], v[24:25] op_sel_hi:[1,0]\n"
    "v_pk_mul_f32 v[22:23], v[22:23], v[24:25] op_sel_hi:[1,0]\n"
    "v_cvt_pk_bf16_f32 v28, v30, v30\n"
    "v_cvt_pk_bf16_f32 v26, v20, v21\n"
    "v_cvt_pk_bf16_f32 v27, v22, v23\n"
    "v_and_b32 v29, v28, %[vm23]\n"
    "v_and_b32 v28, v28, %[vm01]\n"
    "s_nop 1\n"
    "v_permlane32_swap_b32 v26, v28\n"
    "v_permlane32_swap_b32 v27, v29\n"
    "s_nop 1\n"
    "v_mfma_f32_16x16x16_bf16 v[40:43], v[80:81], v[26:27], 0\n"
    "v_mfma_f32_16x16x16_bf16 v[44:47], v[82:83], v[26:27], 0\n"
    "v_mfma_f32_16x16x16_bf16 v[40:43], v[84:85], v[28:29], v[40:43]\n"
    "v_mfma_f32_16x16x16_bf16 v[44:47], v[86:87], v[28:29], v[44:47]\n"
    "s_add_u32 s67, s60, 1\n"
    "s_lshr_b32 s64, s67, 5\n"
    "s_lshr_b32 s64, %[par2], s64\n"
    "s_and_b32 s64, s64, 1\n"
    "s_lshl_b32 s64, s64, 13\n"
    "s_lshl_b32 s65, s67, 16\n"
    "s_add_u32 s65, s65, s64\n"
    "s_add_u32 s65, s65, %[oex]\n"
    "s_waitcnt lgkmcnt(0)\n"
    "s_min_u32 s84, s84, 0xfffffffe\n"
    "v_writelane_b32 v0, s84, 32\n"
    "v_writelane_b32 v1, s84, 32\n"
    "v_add_f32_dpp v40, v44, v40 row_ror:8 row_mask:0xf bank_mask:0xf\n"
    "v_add_f32_dpp v41, v45, v41 row_ror:8 row_mask:0xf bank_mask:0xf\n"
    "v_add_f32_dpp v42, v46, v42 row_ror:8 row_mask:0xf bank_mask:0xf\n"
    "v_add_f32_dpp v43, v47, v43 row_ror:8 row_mask:0xf bank_mask:0xf\n"
    "s_mov_b64 exec, %[m8]\n"
    "ds_write_b128 %[vredw], v[40:43]\n"
    "s_mov_b64 exec, -1\n"
    "s_waitcnt lgkmcnt(0)\n"
    "s_barrier\n"
    "s_cmp_eq_u32 %[w], 0\n"
    "s_cbranch_scc0 LBnopub_%=\n"
    "s_mov_b32 exec_hi, 0\n"
    "ds_read_b128 v[0:3], %[vredr]\n"
    "ds_read_b128 v[4:7], %[vredr] offset:512\n"
    "ds_read_b128 v[8:11], %[vredr] offset:1024\n"
    "ds_read_b128 v[12:15], %[vredr] offset:1536\n"
    "s_waitcnt lgkmcnt(2)\n"
    "v_pk_add_f32 v[0:1], v[0:1], v[4:5]\n"
    "v_pk_add_f32 v[2:3], v[2:3], v[6:7]\n"
    "s_waitcnt lgkmcnt(0)\n"
    "v_pk_add_f32 v[8:9], v[8:9], v[12:13]\n"
    "v_pk_add_f32 v[10:11], v[10:11], v[14:15]\n"
    "v_pk_add_f32 v[0:1], v[0:1], v[8:9]\n"
    "v_pk_add_f32 v[2:3], v[2:3], v[10:11]\n"
    "v_cvt_pk_bf16_f32 v0, v0, v1\n"
    "v_cvt_pk_bf16_f32 v1, v2, v3\n"
    "s_mov_b64 exec, %[mpub]\n"
    "buffer_store_dwordx2 v[0:1], %[vmo], %[rs], s65 offen sc1\n"
    "s_mov_b64 exec, 1\n"
    "v_mov_b32 v4, s60\n"
    "v_mov_b32 v5, 0\n"
    "buffer_store_dword v4, v5, %[rs], s73 offen sc1\n"
    "s_mov_b64 exec, -1\n"
    "LBnopub_%=:\n"
    "s_add_u32 s64, s60, 12\n"
    "s_min_u32 s64, s64, 64\n"
    "s_cmp_ge_u32 s83, s64\n"
    "s_cbranch_scc1 LBq2nf_%=\n"
    "buffer_load_dword v126, %[vfo], %[rs], s71 offen sc1\n"
    "s_mov_b32 s101, 1\n"
    "LBq2nf_%=:\n"
    "LBlast_%=:\n"
    "s_add_u32 s60, s60, 1\n"
    "s_cmp_eq_u32 s60, 64\n"
    "s_cbranch_scc1 LBend_%=\n"
    "s_lshr_b32 s64, s60, 5\n"
    "s_lshr_b32 s64, %[par2], s64\n"
    "s_and_b32 s64, s64, 1\n"
    "s_lshl_b32 s64, s64, 13\n"
    "s_lshl_b32 s61, s60, 16\n"
    "s_add_u32 s61, s61, s64\n"
    "s_add_u32 s61, s61, %[oex]\n"
    "s_branch LBtop_%=\n"
    "LBpmg_%=:\n"
    "s_mov_b32 s62, 0\n"
    "s_branch LBpmd_%=\n"
    "LBpsg_%=:\n"
    "s_mov_b32 s62, 0\n"
    "s_branch LBpsd_%=\n"
    "LBpeg_%=:\n"
    "s_mov_b32 s62, 0\n"
    "s_branch LBped_%=\n"
    "LBend_%=:\n"
    "s_waitcnt vmcnt(0)\n"
    "s_barrier\n"
    "s_cmp_eq_u32 %[w], 0\n"
    "s_cbranch_scc0 LBfin_%=\n"
    "s_mov_b64 exec, 1\n"
    "v_mov_b32 v16, s60\n"
    "v_mov_b32 v17, 0\n"
    "buffer_store_dword v16, v17, %[rs], s73 offen sc1\n"
    "s_mov_b64 exec, -1\n"
    "LBfin_%=:\n"
    "s_waitcnt vmcnt(0)\n"

    :: [rs] "s"(rs), [par2] "s"(par2), [w] "s"(w), [oex] "i"(O_EX), [spin] "i"(SPIN_ASM), [tag] "i"(TAG),
       [sw] "s"(O_BF + (unsigned)((4 * w * 28 + j) * 14) * KB), [svf] "s"(O_VF + (unsigned)(j * 16 + 4 * w) * KB), [sflv] "s"(FL_CV),
       [st2] "s"(O_T2F + (unsigned)(j * 16 + 4 * w) * 256u), [sflb] "s"(FL_CB + (unsigned)j * 256u), [m8] "s"(M8),
       [c0] "i"(PJ_C0), [c1] "i"(PJ_C1), [rr] "i"(PJ_R), [pp] "i"(PJ_P), [r1] "i"(PJ_R1), [pinc] "i"(PJ_PINC), [d1] "i"(PJ_D1), [nd2] "i"(-(PJ_D2)), [pcap] "i"(PJ_PCAP), [pinc2] "i"(PJ_PINC2), [dst] "i"(PJ_DST), [fst] "i"(PJ_FST), [pa0] "i"(PJ_PA0), [mcap] "i"(PJ_MCAP), [psw] "i"(PJ_SW), [pdp] "i"(PJ_DP), [qe] "i"(PJ_QE), [xinc] "i"(PJ_XINC), [xcap] "i"(PJ_XCAP), [qd] "i"(PJ_QD), [th] "i"(PJ_TH), [vstp] "v"(STAMP_OFF + (lane >> 4) * 128u),
       [mpub] "s"(j < 4 ? 0x1FFFFFFFFull : 0xFFFFFFFFull),
       [vl16] "v"(lane * 16), [voa] "v"(voa), [vob] "v"(vob), [vlr] "v"((unsigned)(g * 8 + (n & 7)) * 16u),
       [vfo] "v"(j * 256),
       [vseli] "v"((n >> 3) == (g & 1) ? (n & 7) >> 1 : 99), [vselv] "v"((n & 1) ? 0x3F800000u : 0x00003F80u),
       [veo] "v"((unsigned)(2 * w) * 1024u + lane * 16u),
       [vsto] "v"(49152u + 5120u + (unsigned)(4 * w + 2 * (g >> 1) + (n >> 3)) * 128u + (unsigned)(n & 7) * 8u),
       [vs0] "v"(vs[0]), [vs1] "v"(vs[1]), [vs2] "v"(vs[2]), [vm01] "v"(m01), [vm23] "v"(m23),
       [vredw] "v"(16384u + w * 512u + (unsigned)(g * 8 + (n & 7)) * 16u), [vredr] "v"(16384u + lane * 16u),
       [vmo] "v"(lane < 32 ? (unsigned)((2 * j + ((lane >> 3) >> 1)) * 8 + (lane & 7)) * 16u + ((lane >> 3) & 1) * 8u : STAMP_OFF + (j & 3) * 128u),
       [vt2o] "v"(n * 16u + (g & 1) * 8u),
       [vtch] "v"(w == 0 ? 16384u + lane * 128u : (w == 1 ? (lane < 40 ? 32768u + lane * 128u : 7168u + (unsigned)(lane & 7) * 128u) : 49152u + (unsigned)(lane < 40 ? lane : 0) * 128u)) TS_OPS
    : "v0", "v1", "v2", "v3", "v4", "v5", "v6", "v7", "v8", "v9", "v10", "v11", "v12", "v13", "v14", "v15", "v16", "v17", "v18", "v19", "v20", "v21", "v22", "v23", "v24", "v25", "v26", "v27", "v28", "v29", "v30", "v31", "v32", "v33", "v34", "v35", "v36", "v37", "v38", "v39", "v40", "v41", "v42", "v43", "v44", "v45", "v46", "v47", "v48", "v49", "v50", "v51", "v52", "v53", "v54", "v55", "v56", "v57", "v58", "v59", "v60", "v61", "v62", "v63", "v64", "v65", "v66", "v67", "v68", "v69", "v70", "v71", "v72", "v73", "v74", "v75", "v76", "v77", "v78", "v79", "v80", "v81", "v82", "v83", "v84", "v85", "v86", "v87", "v88", "v89", "v90", "v91", "v92", "v93", "v94", "v95", "v96", "v97", "v98", "v99", "v100", "v101", "v102", "v103", "v104", "v105", "v106", "v107", "v108", "v109", "v110", "v111", "v112", "v113", "v114", "v115", "v116", "v117", "v118", "v119", "v120", "v121", "v122", "v123", "v124", "v125", "v126", "v127", "v128", "v129", "v130", "v131", "v132", "v133", "v134", "v135", "v136", "v137", "v138", "v139", "v140", "v141", "v142", "v143", "a0", "a1", "a2", "a3", "a4", "a5", "a6", "a7", "a8", "a9", "a10", "a11", "a12", "a13", "a14", "a15", "a16", "a17", "a18", "a19", "a20", "a21", "a22", "a23", "a24", "a25", "a26", "a27", "a28", "a29", "a30", "a31", "a32", "a33", "a34", "a35", "a36", "a37", "a38", "a39", "a40", "a41", "a42", "a43", "a44", "a45", "a46", "a47", "a48", "a49", "a50", "a51", "a52", "a53", "a54", "a55", "a56", "a57", "a58", "a59", "a60", "a61", "a62", "a63", "a64", "a65", "a66", "a67", "a68", "a69", "a70", "a71", "a72", "a73", "a74", "a75", "a76", "a77", "a78", "a79", "a80", "a81", "a82", "a83", "a84", "a85", "a86", "a87", "a88", "a89", "a90", "a91", "a92", "a93", "a94", "a95", "a96", "a97", "a98", "a99", "a100", "a101", "a102", "a103", "a104", "a105", "a106", "a107", "a108", "a109", "a110", "a111", "a112", "a113", "a114", "a115", "a116", "a117", "a118", "a119", "a120", "a121", "a122", "a123", "a124", "a125", "a126", "a127", "a128", "a129", "a130", "a131", "a132", "a133", "a134", "a135", "a136", "a137", "a138", "a139", "a140", "a141", "a142", "a143", "a144", "a145", "a146", "a147", "a148", "a149", "a150", "a151", "a152", "a153", "a154", "a155", "a156", "a157", "a158", "a159", "a160", "a161", "a162", "a163", "a164", "a165", "a166", "a167", "a168", "a169", "a170", "a171", "a172", "a173", "a174", "a175", "a176", "a177", "a178", "a179", "a180", "a181", "a182", "a183", "a184", "a185", "a186", "a187", "a188", "a189", "a190", "a191", "a192", "a193", "a194", "a195", "a196", "a197", "a198", "a199", "a200", "a201", "a202", "a203", "a204", "a205", "a206", "a207", "a208", "a209", "a210", "a211", "a212", "a213", "a214", "a215", "a216", "a217", "a218", "a219", "a220", "a221", "a222", "a223", "a224", "a225", "a226", "a227", "a228", "a229", "a230", "a231", "a232", "a233", "a234", "a235", "a236", "a237", "a238", "a239", "s60", "s61", "s62", "s63", "s64", "s65", "s66", "s67", "s68", "s69", "s70", "s71", "s72", "s73", "s74", "s75", "s76", "s77", "s78", "s79", "s80", "s81", "s82", "s83", "s84", "s85", "s86", "s87", "s88", "s89", "s90", "s91", "s92", "s93", "s94", "s95", "s96", "s97", "s98", "s99", "s100", "s101", "vcc", "scc", "memory");
}

template <bool WA, bool FIRST>
DEV void mf8(f4 (&acc)[4][2], const u4& w0, const u4& w1, const u4& w2, const u4& w3, const u4& x0, const u4& x1) {
#define MF_(d, wv, xv) "v_mfma_f32_16x16x32_bf16 %" #d ", " MF_AB(wv, xv) ", " MF_C(d) "\n\t"
#define MF_ALL MF_(0, 8, 12) MF_(1, 8, 13) MF_(2, 9, 12) MF_(3, 9, 13) MF_(4, 10, 12) MF_(5, 10, 13) MF_(6, 11, 12) MF_(7, 11, 13)
    if (FIRST) {
#define MF_C(d) "0"
        if (WA) {
#define MF_AB(wv, xv) "%" #wv ", %" #xv
            asm volatile(MF_ALL : "=&v"(acc[0][0]), "=&v"(acc[0][1]), "=&v"(acc[1][0]), "=&v"(acc[1][1]), "=&v"(acc[2][0]), "=&v"(acc[2][1]), "=&v"(acc[3][0]), "=&v"(acc[3][1])
                         : "a"(w0), "a"(w1), "a"(w2), "a"(w3), "v"(x0), "v"(x1));
#undef MF_AB
        } else {
#define MF_AB(wv, xv) "%" #xv ", %" #wv
            asm volatile(MF_ALL : "=&v"(acc[0][0]), "=&v"(acc[0][1]), "=&v"(acc[1][0]), "=&v"(acc[1][1]), "=&v"(acc[2][0]), "=&v"(acc[2][1]), "=&v"(acc[3][0]), "=&v"(acc[3][1])
                         : "a"(w0), "a"(w1), "a"(w2), "a"(w3), "v"(x0), "v"(x1));
#undef MF_AB
        }
#undef MF_C
    } else {
#define MF_C(d) "%" #d
        if (WA) {
#define MF_AB(wv, xv) "%" #wv ", %" #xv
            asm volatile(MF_ALL : "+v"(acc[0][0]), "+v"(acc[0][1]), "+v"(acc[1][0]), "+v"(acc[1][1]), "+v"(acc[2][0]), "+v"(acc[2][1]), "+v"(acc[3][0]), "+v"(acc[3][1])
                         : "a"(w0), "a"(w1), "a"(w2), "a"(w3), "v"(x0), "v"(x1));
#undef MF_AB
        } else {
#define MF_AB(wv, xv) "%" #xv ", %" #wv
            asm volatile(MF_ALL : "+v"(acc[0][0]), "+v"(acc[0][1]), "+v"(acc[1][0]), "+v"(acc[1][1]), "+v"(acc[2][0]), "+v"(acc[2][1]), "+v"(acc[3][0]), "+v"(acc[3][1])
                         : "a"(w0), "a"(w1), "a"(w2), "a"(w3), "v"(x0), "v"(x1));
#undef MF_AB
        }
#undef MF_C
    }
#undef MF_
#undef MF_ALL
}
template <bool ISG>
DEV void role_GV(const Params& P, rsrc_t rs, unsigned char* lds, int wi, int w, int lane) {
    const int n = lane & 15, g = lane >> 4;
    u4* xL = (u4*)lds;
    const unsigned char* xg = P.ws + O_XF + (unsigned)(w * 7) * KB + lane * 16;
    auto stage = [&](int s) {
#pragma unroll
        for (int i = 0; i < 7; ++i)
            __builtin_amdgcn_global_load_lds((const __attribute__((address_space(1))) void*)(xg + (unsigned)(s * 28 + i) * KB),
                                             (__attribute__((address_space(3))) void*)(lds + ((s % 3) * 28 + w * 7 + i) * KB), 16, 0, 0);
    };
    stage(0);
    __builtin_amdgcn_sched_barrier(0);
    u4 W[4][14];
#pragma unroll
    for (int k = 0; k < 14; ++k)
#pragma unroll
        for (int c = 0; c < 4; ++c) {
            unsigned off;
            if (ISG) { const int unit = 8 * wi + 2 * w + (c >> 1); off = O_AF + (unsigned)((unit * 2 + (c & 1)) * 14 + k) * KB; }
            else { const int tile = (4 * w + c) * 28 + wi; off = O_BF + (unsigned)(tile * 14 + k) * KB; }
            W[c][k] = ld16p(rs, off + lane * 16);
        }
    __builtin_amdgcn_sched_barrier(0);
    stage(1);
    asm volatile("s_waitcnt vmcnt(63)\n\ts_barrier" ::: "memory");
    auto body = [&](int s) __attribute__((always_inline)) {
        const u4* xb = xL + (s % 3) * 28 * 64;
        f4 acc[4][2];
        __builtin_amdgcn_sched_barrier(0);
        GSTAMP(0);
        u4 xq[4][2];
#pragma unroll
        for (int k = 0; k < 3; ++k) { xq[k][0] = xb[k * 64 + lane]; xq[k][1] = xb[(14 + k) * 64 + lane]; }
        const unsigned char* xs = xg + (unsigned)((s + 2 < NSEG ? s + 2 : NSEG - 1) * 28) * KB;
        unsigned char* xd = lds + (((s + 2) % 3) * 28 + w * 7) * KB;
#pragma unroll
        for (int k = 0; k < 14; ++k) {
            __builtin_amdgcn_sched_barrier(0);
            if (k + 3 < 14) { xq[(k + 3) & 3][0] = xb[(k + 3) * 64 + lane]; xq[(k + 3) & 3][1] = xb[(17 + k) * 64 + lane]; }
            if (k >= 2 && k < 9)
                __builtin_amdgcn_global_load_lds((const __attribute__((address_space(1))) void*)(xs + (unsigned)(k - 2) * KB),
                                                 (__attribute__((address_space(3))) void*)(xd + (k - 2) * KB), 16, 0, 0);
            __builtin_amdgcn_sched_barrier(0);
            if (k == 0) {
                if (ISG) mf8<true, true>(acc, W[0][k], W[1][k], W[2][k], W[3][k], xq[k & 3][0], xq[k & 3][1]);
                else mf8<false, true>(acc, W[0][k], W[1][k], W[2][k], W[3][k], xq[k & 3][0], xq[k & 3][1]);
            } else {
                if (ISG) mf8<true, false>(acc, W[0][k], W[1][k], W[2][k], W[3][k], xq[k & 3][0], xq[k & 3][1]);
                else mf8<false, false>(acc, W[0][k], W[1][k], W[2][k], W[3][k], xq[k & 3][0], xq[k & 3][1]);
            }
        }
        asm volatile("s_nop 7\n\ts_nop 7\n\ts_nop 7" : "+v"(acc[0][0]), "+v"(acc[0][1]), "+v"(acc[1][0]), "+v"(acc[1][1]), "+v"(acc[2][0]), "+v"(acc[2][1]), "+v"(acc[3][0]), "+v"(acc[3][1]));
        __builtin_amdgcn_sched_barrier(0);
        GSTAMP(1);
        asm volatile("s_waitcnt vmcnt(7)" ::: "memory");
        GSTAMP(2);
        if (ISG) {
#pragma unroll
            for (int uu = 0; uu < 2; ++uu)
#pragma unroll
                for (int tt = 0; tt < 2; ++tt) {
                    const f4 a = acc[2 * uu][tt], b = acc[2 * uu + 1][tt];
                    const u4 v = {pk2(a[0], a[1]), pk2(a[2], a[3]), pk2(b[0], b[1]), pk2(b[2], b[3])};
                    const int unit = 8 * wi + 2 * w + uu, h = unit / 14, u = unit % 14;
                    st16c(rs, O_GF + (unsigned)(((s * 16 + h) * 2 + tt) * 14 + u) * KB + lane * 16, v);
                }
        } else {
#pragma unroll
            for (int c = 0; c < 4; ++c) {
                const f4 a = acc[c][0], b = acc[c][1];
                const u4 v = {pk2(a[0], a[1]), pk2(a[2], a[3]), pk2(b[0], b[1]), pk2(b[2], b[3])};
                st16c(rs, O_VF + (unsigned)(s * 448 + 16 * wi + 4 * w + c) * KB + lane * 16, v);
            }
        }
        const bool eager = s < GV_EAGER || s == NSEG - 1;
        if (eager) vm0();
        GSTAMP(3);
        asm volatile("s_barrier" ::: "memory");
        GSTAMP(4);
        if (w == 0) TS(blockIdx.x, s);
        if (w == 0 && lane == 0) st4c(rs, (ISG ? FL_CG : FL_CV) + wi * 256, eager ? (unsigned)(s + 1) : (unsigned)(s + 1 - GV_LAG > GV_EAGER ? s + 1 - GV_LAG : GV_EAGER));
    };
    body(0);
    for (int s = 1; s < NSEG; ++s) body(s);
}

DEV void task_fft1(const Params& P, rsrc_t rs, int id, int w, int lane) {
    const int n = lane & 15, g = lane >> 4;
    const int ktg = id >> 4, tr = id & 15, kt = 4 * ktg + w;
    if (kt < 15) {
        u4 F[2][2][14];
#pragma unroll
        for (int ri = 0; ri < 2; ++ri)
#pragma unroll
            for (int hl = 0; hl < 2; ++hl)
#pragma unroll
                for (int ks = 0; ks < 14; ++ks) F[ri][hl][ks] = ld16p(rs, O_FW + (unsigned)(((kt * 2 + ri) * 2 + hl) * 14 + ks) * KB + lane * 16);
        for (int tile = 0; tile < 8; ++tile) {
            const int t0 = tr * 128 + tile * 16;
            u4 X[14];
#pragma unroll
            for (int ks = 0; ks < 14; ++ks) X[ks] = ld16p(rs, O_XF + (unsigned)((t0 >> 4) * 14 + ks) * KB + lane * 16);
            f4 re = {0, 0, 0, 0}, im = {0, 0, 0, 0};
#pragma unroll
            for (int ks = 0; ks < 14; ++ks) {
                re = mfma32(X[ks], F[0][0][ks], re); im = mfma32(X[ks], F[1][0][ks], im);
                re = mfma32(X[ks], F[0][1][ks], re); im = mfma32(X[ks], F[1][1][ks], im);
            }
            const unsigned off = O_Y1 + (unsigned)((16 * kt + n) * 2048 + t0 + 4 * g) * 4u;
            st16c(rs, off, __builtin_bit_cast(u4, re));
            st16c(rs, off + 240 * 2048 * 4, __builtin_bit_cast(u4, im));
        }
    }
    vm0();
    bar();
    if (w == 0 && lane == 0) st4c(rs, FL_1 + id * 4, TAG);
}

DEV int zp(int i) { return i + (i >> 3); }
constexpr int ZC = 2048 + 256;
DEV f2 cmul(f2 a, f2 b) { return f2{a.x * b.x - a.y * b.y, a.x * b.y + a.y * b.x}; }
DEV f2 cmulc(f2 a, f2 b) { return f2{a.x * b.x + a.y * b.y, a.y * b.x - a.x * b.y}; }
template <int R, int S0, bool INV>
DEV void fft_pass(f2* Z, const f2* TW, int tid) {
    constexpr int NE = 1 << R;
    constexpr int LGS = 11 - S0 - R;
    constexpr int STR = 1 << LGS;
    constexpr int NG = 2048 / NE;
    for (int gi = tid; gi < NG; gi += 256) {
        const int lo = gi & (STR - 1), hi = gi >> LGS;
        const int base = (hi << (LGS + R)) + lo;
        f2 tw[R][NE / 2];
#pragma unroll
        for (int q = 0; q < R; ++q) {
            const int half = NE >> (q + 1);
#pragma unroll
            for (int jj = 0; jj < half; ++jj) {
                const int p = lo + jj * STR;
                tw[q][jj] = TW[p << (S0 + q)];
            }
        }
#pragma unroll
        for (int col = 0; col < 4; ++col) {
            f2* z = Z + col * ZC;
            f2 v[NE];
#pragma unroll
            for (int j = 0; j < NE; ++j) v[j] = z[zp(base + j * STR)];
            if (!INV) {
#pragma unroll
                for (int q = 0; q < R; ++q) {
                    const int half = NE >> (q + 1);
#pragma unroll
                    for (int blk = 0; blk < NE; blk += 2 * half)
#pragma unroll
                        for (int jj = 0; jj < half; ++jj) {
                            const f2 a = v[blk + jj], b = v[blk + jj + half];
                            v[blk + jj] = a + b;
                            v[blk + jj + half] = cmul(a - b, tw[q][jj]);
                        }
                }
            } else {
#pragma unroll
                for (int q = R - 1; q >= 0; --q) {
                    const int half = NE >> (q + 1);
#pragma unroll
                    for (int blk = 0; blk < NE; blk += 2 * half)
#pragma unroll
                        for (int jj = 0; jj < half; ++jj) {
                            const f2 a = v[blk + jj], b = cmulc(v[blk + jj + half], tw[q][jj]);
                            v[blk + jj] = a + b;
                            v[blk + jj + half] = a - b;
                        }
                }
            }
#pragma unroll
            for (int j = 0; j < NE; ++j) z[zp(base + j * STR)] = v[j];
        }
    }
}
DEV void task_fft2(const Params& P, rsrc_t rs, unsigned char* lds, int c, int tid, int w, int lane, bool& alive) {
    f2* Z = (f2*)lds;
    f2* TW = Z + 4 * ZC;
    for (int i = tid; i < 1024; i += 256) { float sv, cv; sincospif((float)i * (1.0f / 1024.0f), &sv, &cv); TW[i] = f2{cv, -sv}; }
    if (w == 0 && alive) alive = wait_flags(rs, FL_1 + (c >> 4) * 64, 16, lane);
    bar();
    for (int col = 0; col < 4; ++col) {
        const int k = 4 * c + col;
#pragma unroll
        for (int i = 0; i < 2; ++i) {
            const int t = (tid + 256 * i) * 4;
            const u4 re = ld16c(rs, O_Y1 + (unsigned)(k * 2048 + t) * 4u), im = ld16c(rs, O_Y1 + (unsigned)((240 + k) * 2048 + t) * 4u);
            f2* z = Z + col * ZC;
            z[zp(t + 0)] = f2{fu(re.x), fu(im.x)}; z[zp(t + 1)] = f2{fu(re.y), fu(im.y)};
            z[zp(t + 2)] = f2{fu(re.z), fu(im.z)}; z[zp(t + 3)] = f2{fu(re.w), fu(im.w)};
        }
    }
    bar();
    fft_pass<3, 0, false>(Z, TW, tid); bar();
    fft_pass<3, 3, false>(Z, TW, tid); bar();
    fft_pass<3, 6, false>(Z, TW, tid); bar();
    fft_pass<2, 9, false>(Z, TW, tid); bar();
    for (int i = 0; i < 8; ++i) {
        const int p = tid + 256 * i;
        const u4 wv = ld16p(rs, O_WT + (unsigned)(c * 2048 + p) * 16u);
        const unsigned ww[4] = {wv.x, wv.y, wv.z, wv.w};
#pragma unroll
        for (int col = 0; col < 4; ++col) {
            f2* z = Z + col * ZC + zp(p);
            *z = cmul(*z, f2{bflo(ww[col]), bfhi(ww[col])});
        }
    }
    bar();
    fft_pass<2, 9, true>(Z, TW, tid); bar();
    fft_pass<3, 6, true>(Z, TW, tid); bar();
    fft_pass<3, 3, true>(Z, TW, tid); bar();
    fft_pass<3, 0, true>(Z, TW, tid); bar();
    for (int i = 0; i < 8; ++i) {
        const int t = tid + 256 * i;
        unsigned hi[4], lo[4];
#pragma unroll
        for (int col = 0; col < 4; ++col) {
            const f2 v = Z[col * ZC + zp(t)];
            hi[col] = pk2(v.x, v.y);
            lo[col] = pk2(v.x - bflo(hi[col]), v.y - bfhi(hi[col]));
        }
        const unsigned off = O_Y2 + (unsigned)(((t >> 4) * 15 + (c >> 2)) * 2) * KB + (unsigned)((c & 3) * 16 + (t & 15)) * 16u;
        st16c(rs, off, u4{hi[0], hi[1], hi[2], hi[3]});
        st16c(rs, off + KB, u4{lo[0], lo[1], lo[2], lo[3]});
    }
    vm0();
    bar();
    if (w == 0 && lane == 0) st4c(rs, FL_2 + c * 4, TAG);
}

DEV void task_x1(const Params& P, rsrc_t rs, int s, int q, int w, int lane, bool& alive) {
    const int n = lane & 15, g = lane >> 4, h = 4 * q + w;
    if (alive) alive = wait_cnt(rs, FL_CG, 28, s + 1, lane);
    f4 a00 = {0, 0, 0, 0}, a10 = {0, 0, 0, 0}, a11 = {0, 0, 0, 0};
#pragma unroll
    for (int u = 0; u < 14; ++u) {
        const u4 g0 = ld16c(rs, O_GF + (unsigned)(((s * 16 + h) * 2 + 0) * 14 + u) * KB + lane * 16);
        const u4 g1 = ld16c(rs, O_GF + (unsigned)(((s * 16 + h) * 2 + 1) * 14 + u) * KB + lane * 16);
        const u4 x0 = ld16p(rs, O_XF + (unsigned)(s * 28 + u) * KB + lane * 16);
        const u4 x1 = ld16p(rs, O_XF + (unsigned)(s * 28 + 14 + u) * KB + lane * 16);
        a00 = mfma32(g0, x0, a00); a10 = mfma32(g0, x1, a10); a11 = mfma32(g1, x1, a11);
    }
    const unsigned off = O_SXX + (unsigned)((s * 16 + h) * 2) * 2048u + lane * 32u;
    const u4 z = {0, 0, 0, 0};
    st16c(rs, off, __builtin_bit_cast(u4, a00)); st16c(rs, off + 16, z);
    st16c(rs, off + 2048, __builtin_bit_cast(u4, a10)); st16c(rs, off + 2048 + 16, __builtin_bit_cast(u4, a11));
    vm0();
    bar();
    if (w == 0 && lane == 0) st4c(rs, FL_X1 + s * 128 + q * 4, TAG);
}

DEV void allg(float v, float o[4]) {
    auto r = __builtin_amdgcn_permlane16_swap(uf(v), uf(v), false, false);
    auto a = __builtin_amdgcn_permlane32_swap(r[0], r[0], false, false);
    auto b = __builtin_amdgcn_permlane32_swap(r[1], r[1], false, false);
    o[0] = fu(a[0]); o[2] = fu(a[1]); o[1] = fu(b[0]); o[3] = fu(b[1]);
}

template <int NT>
DEV void task_late(const Params& P, rsrc_t rs, unsigned char* lds, int s, int q, int t0, int tid, int w, int lane, bool& alive) {
    const int n = lane & 15, g = lane >> 4;
    u4* YL = (u4*)lds;
    float* OB = (float*)(lds + 61440);
    float* OA = (float*)lds;
    const float gt0 = *(const float*)(P.ws + O_GATE + (32 * s + 16 * t0 + tid / 14) * 4), gt1 = *(const float*)(P.ws + O_GATE + (32 * s + ((tid + 256) / 14 & 31)) * 4);
    if (w == 0 && alive) alive = wait_flags(rs, FL_2, 57, lane);
    bar();
    for (int i = tid; i < 30 * NT * 64; i += 256) YL[i] = ld16c(rs, O_Y2 + (unsigned)(2 * s + t0) * 30u * KB + i * 16u);
    bar();
    {
        for (int fl = w; fl < 7; fl += 4) {
            const int ft = 7 * q + fl;
            f4 acc[NT];
#pragma unroll
            for (int tt = 0; tt < NT; ++tt) acc[tt] = f4{0, 0, 0, 0};
#pragma unroll 5
            for (int u = 0; u < 15; ++u) {
                const u4 fh = ld16p(rs, O_FI + (unsigned)((ft * 2 + 0) * 15 + u) * KB + lane * 16), fl2 = ld16p(rs, O_FI + (unsigned)((ft * 2 + 1) * 15 + u) * KB + lane * 16);
#pragma unroll
                for (int tt = 0; tt < NT; ++tt) {
                    const u4 yh = YL[((tt * 15 + u) * 2 + 0) * 64 + lane], yl = YL[((tt * 15 + u) * 2 + 1) * 64 + lane];
                    acc[tt] = mfma32(fh, yh, acc[tt]); acc[tt] = mfma32(fl2, yh, acc[tt]); acc[tt] = mfma32(fh, yl, acc[tt]);
                }
            }
#pragma unroll
            for (int tt = 0; tt < NT; ++tt) *(f4*)(OB + (16 * tt + n) * 112 + 16 * fl + 4 * g) = acc[tt];
        }
    }
    if (alive) alive = wait_cnt(rs, FL_CV + 7 * q * 256, 7, s + 1, lane);
    u4 V[4][7];
#pragma unroll
    for (int hh = 0; hh < 4; ++hh)
#pragma unroll
        for (int fl = 0; fl < 7; ++fl) V[hh][fl] = ld16c(rs, O_VF + (unsigned)(s * 448 + (7 * q + fl) * 16 + 4 * w + hh) * KB + lane * 16);
    if (alive) alive = wait_flags(rs, FL_X1 + s * 128, 4, lane);
    f4 SX[4][NT][2];
#pragma unroll
    for (int hh = 0; hh < 4; ++hh)
#pragma unroll
        for (int tt = 0; tt < NT; ++tt) {
            const unsigned off = O_SXX + (unsigned)((s * 16 + 4 * w + hh) * 2 + t0 + tt) * 2048u + lane * 32u;
            SX[hh][tt][0] = __builtin_bit_cast(f4, ld16c(rs, off)); SX[hh][tt][1] = __builtin_bit_cast(f4, ld16c(rs, off + 16));
        }
    float mx0[NT][4], lx0[NT][4];
#pragma unroll
    for (int tt = 0; tt < NT; ++tt)
#pragma unroll
        for (int hh = 0; hh < 4; ++hh) {
            float v[8];
#pragma unroll
            for (int jj = 0; jj < 8; ++jj) {
                const int tp = 16 * (jj >> 2) + 4 * g + (jj & 3);
                v[jj] = tp <= 16 * (t0 + tt) + n ? SX[hh][tt][jj >> 2][jj & 3] : -3.0e38f;
            }
            float mx = fmaxf(fmaxf(fmaxf(v[0], v[1]), fmaxf(v[2], v[3])), fmaxf(fmaxf(v[4], v[5]), fmaxf(v[6], v[7])));
            mx = x32max(x16max(mx));
            float l = 0.f;
#pragma unroll
            for (int jj = 0; jj < 8; ++jj) { const float e = ex2(v[jj] - mx); SX[hh][tt][jj >> 2][jj & 3] = e; l += e; }
            mx0[tt][hh] = mx; lx0[tt][hh] = x32add(x16add(l));
        }
    if (w == 0 && s >= 60) TS(blockIdx.x, 19);
    if (alive) alive = wait_cnt(rs, FL_CB + 7 * q * 256, 7, s + 1, lane, true);
    if (w == 0 && s >= 60) TS(blockIdx.x, 20);
    if (alive) alive = wait_cnt2(rs, FL_CA, FL_CB + 7 * q * 256, s + 1, lane);
    u4 T2[7];
#pragma unroll
    for (int fl = 0; fl < 7; ++fl) T2[fl] = ld16c(rs, O_T2F + (unsigned)((s * 28 + 7 * q + fl) * 16 + 4 * w + g) * 256u + n * 16u);
    if (w == 0 && s >= 60) TS(blockIdx.x, 21);
    f4 SM[NT][2];
#pragma unroll
    for (int tt = 0; tt < NT; ++tt) {
        f4 a = {0, 0, 0, 0}, b = {0, 0, 0, 0};
#pragma unroll
        for (int cj = 0; cj < 5; ++cj) {
            const unsigned off = O_SXM + (unsigned)((s * 16 + 4 * w + g) * 5 + cj) * KB + (unsigned)(((t0 + tt) * 16 + n) * 8) * 4u;
            a += __builtin_bit_cast(f4, ld16c(rs, off)); b += __builtin_bit_cast(f4, ld16c(rs, off + 16));
        }
        SM[tt][0] = a; SM[tt][1] = b;
    }
    if (w == 0 && s >= 60) TS(blockIdx.x, 22);
    u4 PX[4][NT], PM[NT];
#pragma unroll
    for (int tt = 0; tt < NT; ++tt) {
        float mm = fmaxf(fmaxf(fmaxf(SM[tt][0][0], SM[tt][0][1]), fmaxf(SM[tt][0][2], SM[tt][0][3])), fmaxf(fmaxf(SM[tt][1][0], SM[tt][1][1]), fmaxf(SM[tt][1][2], SM[tt][1][3])));
        float mmb[4]; allg(mm, mmb);
        float m[4], lx[4], al[4];
#pragma unroll
        for (int hh = 0; hh < 4; ++hh) {
            m[hh] = fmaxf(mx0[tt][hh], mmb[hh]);
            al[hh] = ex2(mx0[tt][hh] - m[hh]);
            lx[hh] = lx0[tt][hh] * al[hh];
        }
        const float mo = g == 0 ? m[0] : (g == 1 ? m[1] : (g == 2 ? m[2] : m[3]));
        float em[8], lm = 0.f;
#pragma unroll
        for (int jj = 0; jj < 8; ++jj) { em[jj] = ex2(SM[tt][jj >> 2][jj & 3] - mo); lm += em[jj]; }
        float lmb[4]; allg(lm, lmb);
        float ri[4];
#pragma unroll
        for (int hh = 0; hh < 4; ++hh) {
            ri[hh] = 1.0f / (lx[hh] + lmb[hh]);
            const float c = al[hh] * ri[hh];
            const f4 e0 = SX[hh][tt][0] * c, e1 = SX[hh][tt][1] * c;
            PX[hh][tt] = u4{pk2(e0[0], e0[1]), pk2(e0[2], e0[3]), pk2(e1[0], e1[1]), pk2(e1[2], e1[3])};
        }
        const float ro = g == 0 ? ri[0] : (g == 1 ? ri[1] : (g == 2 ? ri[2] : ri[3]));
        PM[tt] = u4{pk2(em[0] * ro, em[1] * ro), pk2(em[2] * ro, em[3] * ro), pk2(em[4] * ro, em[5] * ro), pk2(em[6] * ro, em[7] * ro)};
    }
    if (w == 0 && s >= 60) TS(blockIdx.x, 23);
    bar();
#pragma unroll
    for (int fl = 0; fl < 7; ++fl)
#pragma unroll
        for (int tt = 0; tt < NT; ++tt) {
            f4 a = mfma32(T2[fl], PM[tt], f4{0, 0, 0, 0});
#pragma unroll
            for (int hh = 0; hh < 4; ++hh) a = mfma32(V[hh][fl], PX[hh][tt], a);
            *(f4*)(OA + (w * 32 + 16 * tt + n) * 112 + 16 * fl + 4 * g) = a;
        }
    bar();
    if (w == 0 && s >= 60) TS(blockIdx.x, 24);
    for (int i = tid; i < 16 * NT * 14; i += 256) {
        const int t = i / 14, ch = i % 14;
        const float gt = i < 256 ? gt0 : gt1;
        const int ix = t * 112 + ch * 8;
        f4 a2[2], b2[2];
#pragma unroll
        for (int z = 0; z < 2; ++z) {
            const f4 p0 = *(const f4*)(OA + ix + 4 * z), p1 = *(const f4*)(OA + 3584 + ix + 4 * z), p2 = *(const f4*)(OA + 2 * 3584 + ix + 4 * z), p3 = *(const f4*)(OA + 3 * 3584 + ix + 4 * z);
            a2[z] = (p0 + p1) + (p2 + p3);
            b2[z] = *(const f4*)(OB + ix + 4 * z);
        }
        unsigned o[4];
#pragma unroll
        for (int e2 = 0; e2 < 4; ++e2) {
            float r[2];
#pragma unroll
            for (int z = 0; z < 2; ++z) {
                const int f = 2 * e2 + z;
                const float ab = bflo(pk2(a2[f >> 2][f & 3], 0.f));
                const float bb = bflo(pk2(b2[f >> 2][f & 3] * (1.0f / (2048.0f * 448.0f)), 0.f));
                r[z] = gt * ab + (1.0f - gt) * bb;
            }
            o[e2] = pk2(r[0], r[1]);
        }
        *(u4*)(P.out + (size_t)(32 * s + 16 * t0 + t) * DM + 112 * q + ch * 8) = u4{o[0], o[1], o[2], o[3]};
    }
    bar();
}

DEV void role_general(const Params& P, rsrc_t rs, unsigned char* lds, int wk, int tid, int w, int lane) {
    bool alive = true;
    if (w == 0) TS(blockIdx.x, 0);
    if (wk < 64) task_fft1(P, rs, wk, w, lane);
    if (w == 0) TS(blockIdx.x, 1);
    if (wk < 57) task_fft2(P, rs, lds, wk, tid, w, lane, alive);
    if (w == 0) TS(blockIdx.x, 2);
    for (int id = wk; id < 520; id += NGW) {
        const int s = id >> 3, r = id & 7;
        if (id >= 512) task_late<1>(P, rs, lds, id < 516 ? 63 : 62, id & 3, 1, tid, w, lane, alive);
        else if (s >= 62 && r >= 4) task_late<1>(P, rs, lds, s, r - 4, 0, tid, w, lane, alive);
        else if (r < 4) task_x1(P, rs, s, r, w, lane, alive);
        else task_late<2>(P, rs, lds, s, r - 4, 0, tid, w, lane, alive);
        if (w == 0) TS(blockIdx.x, 3 + id / NGW);
    }
}

__global__ void __launch_bounds__(256, 1) main_kernel(Params P) {
    extern __shared__ __attribute__((aligned(16))) unsigned char lds[];
    const int b = blockIdx.x, tid = threadIdx.x, lane = tid & 63, w = __builtin_amdgcn_readfirstlane(tid >> 6);
    rsrc_t rs = __builtin_amdgcn_make_buffer_rsrc(P.ws, 0, 0x10000000, 0x00020000);
    const unsigned par2 = __builtin_amdgcn_readfirstlane((*(const unsigned*)(P.ws + O_PAR) & 1u) | ((*(const unsigned*)(P.ws + O_PAR + 4) & 1u) << 1));
    const int xcd = b & 7, slot = b >> 3;
    if (w == 0) TS(b, 4000);
    if (xcd < 4) {
        const int ci = slot * 4 + xcd;
        if (ci < NA) role_A(P, lds, ci, par2, w, lane);
        else if (ci < NA + NX) role_X(P, ci - NA, par2, w, lane);
        else if (ci < NA + NX + NB) role_B(P, ci - NA - NX, par2, w, lane);
        else if (72 + ci - (NA + NX + NB) < NGW) role_general(P, rs, lds, 72 + ci - (NA + NX + NB), tid, w, lane);
    } else {
        const int fi = slot * 4 + xcd - 4;
        if (fi < 28) role_GV<true>(P, rs, lds, fi, w, lane);
        else if (fi < NGV) role_GV<false>(P, rs, lds, fi - 28, w, lane);
        else {
            role_general(P, rs, lds, fi - NGV, tid, w, lane);
        }
    }
}

extern "C" void kernel_launch(void* const* d_in, const int* in_sizes, int n_in, void* d_out, int out_size, void* d_ws, size_t ws_size,
                              hipStream_t stream) {
    (void)in_sizes; (void)n_in; (void)out_size; (void)ws_size;
    Params P;
    P.x = (const us*)d_in[0]; P.mem0 = (const us*)d_in[1]; P.wq = (const us*)d_in[2]; P.wk = (const us*)d_in[3];
    P.wv = (const us*)d_in[4]; P.wo = (const us*)d_in[5]; P.wr = (const us*)d_in[8]; P.wi = (const us*)d_in[9];
    P.gw = (const us*)d_in[10]; P.out = (us*)d_out; P.ws = (unsigned char*)d_ws;
    (void)hipFuncSetAttribute((const void*)prep_kernel, hipFuncAttributeMaxDynamicSharedMemorySize, PREP_LDS);
    (void)hipFuncSetAttribute((const void*)main_kernel, hipFuncAttributeMaxDynamicSharedMemorySize, LDS_BYTES);
    prep_kernel<<<256, 256, PREP_LDS, stream>>>(P);
    main_kernel<<<256, 256, LDS_BYTES, stream>>>(P);
}
```
